```python
import torch

def _softmax(x, axis=-1):
    return torch.softmax(x, dim=axis)
def _gelu(x):
    return torch.nn.functional.gelu(x, approximate="tanh")
def _silu(x):
    return torch.nn.functional.silu(x)
def _sigmoid(x):
    return torch.sigmoid(x)
def _layernorm(x, axis=-1, eps=1e-5):
    m = x.mean(dim=axis, keepdim=True)
    v = x.var(dim=axis, keepdim=True, unbiased=False)
    return (x - m) / torch.sqrt(v + eps)
def _scatter_add(tgt, idx, src, axis=0):
    return tgt.clone().index_add_(axis, idx, src)
def _segment_sum(src, seg, n):
    out = torch.zeros((n,) + src.shape[1:], dtype=src.dtype)
    return out.index_add_(0, seg, src)
def _pad(x, pw):
    flat = tuple(v for pair in reversed(pw) for v in pair)
    return torch.nn.functional.pad(x, flat)

kernel_name = 'concat_heads__linear_kernel_hedgehog__spectral_fnet__prompt'

def setup_inputs():

    _w = (lambda a: (lambda t: t.to(torch.bfloat16) if t.is_floating_point() else t)(torch.as_tensor(a)))
    def _A_setup():
        B, S, D, H = 1, 2048, 512, 32
        Dh = D // H
        return {
            "x": _w((lambda *s: torch.randn(*s, dtype=torch.bfloat16) * 0.2)(B, S, D)),
            "wqkv": _w((lambda *s: torch.randn(*s, dtype=torch.bfloat16) * 1.6)(D, 3 * D)),
            "w_phi": _w((lambda *s: torch.randn(*s, dtype=torch.bfloat16) * 1.6)(H, Dh, Dh) * (Dh ** -0.5)),
            "wo": _w((lambda *s: torch.randn(*s, dtype=torch.bfloat16) * 0.8)(D, D)),
        }
    _A_in = _A_setup()

    def _B_setup():
        B, S, D = 1, 2048, 512
        ns = torch.arange(S)[:, None] * torch.arange(S)[None, :]
        nd = torch.arange(D)[:, None] * torch.arange(D)[None, :]
        Ws_re = torch.cos(-2.0 * torch.pi * ns / S).to(float)
        Ws_im = torch.sin(-2.0 * torch.pi * ns / S).to(float)
        Wd_re = torch.cos(-2.0 * torch.pi * nd / D).to(float)
        Wd_im = torch.sin(-2.0 * torch.pi * nd / D).to(float)
        return {
            "x": _w((lambda *s: torch.randn(*s, dtype=torch.bfloat16) * 0.05)(B, S, D)),
            "dft_seq_re": _w(Ws_re),
            "dft_seq_im": _w(Ws_im),
            "dft_hid_re": _w(Wd_re),
            "dft_hid_im": _w(Wd_im),
        }
    _B_in = _B_setup()

    return {
        "A_x": _A_in["x"],
        "A_wqkv": _A_in["wqkv"],
        "A_w_phi": _A_in["w_phi"],
        "A_wo": _A_in["wo"],
        "B_x": _B_in["x"],
        "B_dft_seq_re": _B_in["dft_seq_re"],
        "B_dft_seq_im": _B_in["dft_seq_im"],
        "B_dft_hid_re": _B_in["dft_hid_re"],
        "B_dft_hid_im": _B_in["dft_hid_im"],
    }

def reference(A_x, A_wqkv, A_w_phi, A_wo, B_x, B_dft_seq_re, B_dft_seq_im, B_dft_hid_re, B_dft_hid_im):
    def _A_ref(x, wqkv, w_phi, wo):
        B, S, D = x.shape
        H = 32; Dh = D // H
        qkv = (lambda x, *d: x.permute(*d))((x @ wqkv).reshape(B, S, 3, H, Dh), 2, 0, 3, 1, 4)
        q, k, v = qkv[0], qkv[1], qkv[2]
        pq = torch.einsum("bhsd,hdf->bhsf", q, w_phi)
        pk = torch.einsum("bhsd,hdf->bhsf", k, w_phi)
        phi_q = torch.concatenate([_softmax(pq, axis=-1), _softmax(-pq, axis=-1)], axis=-1)
        phi_k = torch.concatenate([_softmax(pk, axis=-1), _softmax(-pk, axis=-1)], axis=-1)
        kv = torch.einsum("bhsf,bhsd->bhfd", phi_k, v)
        z  = torch.einsum("bhsf->bhf", phi_k)
        num = torch.einsum("bhqf,bhfd->bhqd", phi_q, kv)
        den = torch.einsum("bhqf,bhf->bhq", phi_q, z)[..., None] + 1e-6
        o = num / den
        return (lambda x, *d: x.permute(*d))(o, 0, 2, 1, 3).reshape(B, S, D) @ wo

    def _B_ref(x, dft_seq_re, dft_seq_im, dft_hid_re, dft_hid_im):
        a = torch.einsum("bsd,ed->bse", x, dft_hid_re)
        b = torch.einsum("bsd,ed->bse", x, dft_hid_im)
        y = torch.einsum("ts,bse->bte", dft_seq_re, a) - torch.einsum("ts,bse->bte", dft_seq_im, b)
        return y

    a = _A_ref(A_x, A_wqkv, A_w_phi, A_wo)
    b = _B_ref(B_x, B_dft_seq_re, B_dft_seq_im, B_dft_hid_re, B_dft_hid_im)
    return torch.concatenate([a, b], axis=-1)
```

```cpp
#include <hip/hip_runtime.h>
#include <stdint.h>

typedef __bf16 bf16x8 __attribute__((ext_vector_type(8)));
typedef __bf16 bf16x2 __attribute__((ext_vector_type(2)));
typedef float f32x2 __attribute__((ext_vector_type(2)));
typedef float f32x4 __attribute__((ext_vector_type(4)));
typedef float f32x16 __attribute__((ext_vector_type(16)));
typedef unsigned u32x4 __attribute__((ext_vector_type(4)));
typedef unsigned u32x2 __attribute__((ext_vector_type(2)));
typedef short s16x4 __attribute__((ext_vector_type(4)));
#define LDS_AS __attribute__((address_space(3)))
#define GLB_AS __attribute__((address_space(1)))

constexpr size_t WS_PP   = 0;
constexpr size_t WS_PHIQ = (size_t)8 << 20;
constexpr size_t WS_KVP  = (size_t)12 << 20;
constexpr size_t WS_WD   = (size_t)16 << 20;
constexpr size_t WS_WO   = (size_t)17 << 20;
constexpr size_t WS_KVZ  = ((size_t)17 << 20) + ((size_t)512 << 10);
constexpr size_t WS_FLAG = (size_t)18 << 20;

constexpr int NS = 4;
constexpr int STAGE_B = 32768;
constexpr int K1_LDS = NS * STAGE_B + 32768;
constexpr int NT_A = 8, NT_B = 16, NT = NT_A + NT_B;

__device__ __forceinline__ unsigned pk2(float a, float b) {
    f32x2 v = {a, b};
    return __builtin_bit_cast(unsigned, __builtin_convertvector(v, bf16x2));
}
__device__ __forceinline__ float bflo(unsigned u) { return __builtin_bit_cast(float, u << 16); }
__device__ __forceinline__ float bfhi(unsigned u) { return __builtin_bit_cast(float, u & 0xffff0000u); }
__device__ __forceinline__ f32x16 mfma32(u32x4 a, u32x4 b, f32x16 c) {
    return __builtin_amdgcn_mfma_f32_32x32x16_bf16(__builtin_bit_cast(bf16x8, a), __builtin_bit_cast(bf16x8, b), c, 0, 0, 0);
}
__device__ __forceinline__ u32x4 lds_r128(unsigned addr) { return *(const LDS_AS u32x4*)(uintptr_t)addr; }
__device__ __forceinline__ u32x2 lds_tr64(unsigned addr) {
    s16x4 v = __builtin_amdgcn_ds_read_tr16_b64_v4i16((LDS_AS s16x4*)(uintptr_t)addr);
    return __builtin_bit_cast(u32x2, v);
}
__device__ __forceinline__ u32x4 lds_trfrag(unsigned addr) {
    u32x2 lo = lds_tr64(addr), hi = lds_tr64(addr + 512);
    u32x4 r = {lo[0], lo[1], hi[0], hi[1]};
    return r;
}
__device__ __forceinline__ void glds16(const char* g, unsigned lds) {
    __builtin_amdgcn_global_load_lds((const GLB_AS void*)g, (LDS_AS void*)(uintptr_t)lds, 16, 0, 0);
}
#define BARRIER() asm volatile("s_barrier" ::: "memory")
#define WAIT_LGKM0_BARRIER() asm volatile("s_waitcnt lgkmcnt(0)\n\ts_barrier" ::: "memory")
#define WAIT_VM(n) asm volatile("s_waitcnt vmcnt(" #n ")" ::: "memory")

__device__ __forceinline__ void softmax16(const f32x16& p, u32x4& o0, u32x4& o1) {
    float x[16];
#pragma unroll
    for (int i = 0; i < 8; ++i) { unsigned u = pk2(p[2 * i], p[2 * i + 1]); x[2 * i] = bflo(u); x[2 * i + 1] = bfhi(u); }
    float m = x[0];
#pragma unroll
    for (int i = 1; i < 16; ++i) m = fmaxf(m, x[i]);
    float s = 0.f;
#pragma unroll
    for (int i = 0; i < 16; ++i) { x[i] = __builtin_amdgcn_exp2f((x[i] - m) * 1.4426950408889634f); s += x[i]; }
    float inv = __builtin_amdgcn_rcpf(s);
#pragma unroll
    for (int i = 0; i < 4; ++i) { o0[i] = pk2(x[2 * i] * inv, x[2 * i + 1] * inv); o1[i] = pk2(x[8 + 2 * i] * inv, x[9 + 2 * i] * inv); }
}
__device__ __forceinline__ u32x4 pack8(const f32x16& a, int o) {
    u32x4 r;
#pragma unroll
    for (int i = 0; i < 4; ++i) r[i] = pk2(a[o + 2 * i], a[o + 2 * i + 1]);
    return r;
}

__global__ __launch_bounds__(512, 2) void k1_kernel(
    const char* __restrict__ xA, const char* __restrict__ wqkv, const unsigned short* __restrict__ wphi,
    const unsigned short* __restrict__ wo, const char* __restrict__ xB, const char* __restrict__ wsre,
    const char* __restrict__ wsim, const char* __restrict__ wdre, const char* __restrict__ wdim, char* __restrict__ ws)
{
    extern __shared__ __attribute__((aligned(16))) char shm[];
    (void)shm;
    const int tid = threadIdx.x, l = tid & 63;
    const int w = __builtin_amdgcn_readfirstlane(tid >> 6);
    const int b = blockIdx.x, xcd = b & 7, idx = b >> 3;
    const int tsel = idx >> 3;
    const int tm = (tsel >> 1) * 16 + 2 * xcd + (tsel & 1);
    const int tn = (idx >> 1) & 3, kh = idx & 1;
    const int rb = 4 * xcd + (idx >> 3), hq = idx & 7;

    if (w >= 4) {
        const int lw = w - 4;
        const unsigned r8 = l >> 3, swT = (unsigned)(((l & 7) ^ (((l >> 4) & 1) << 2)) << 4);
        const unsigned swR0 = (unsigned)(((l & 7) ^ (l >> 4)) << 4), swR1 = (unsigned)(((l & 7) ^ ((l >> 4) + 4)) << 4);
        const char* aR = xA + (size_t)(64 * rb) * 1024;
        const char* aT = wqkv + (size_t)(64 * hq) * 2;
        const unsigned aRv0 = (16 * lw + r8) * 1024 + swR0, aRv1 = (16 * lw + 8 + r8) * 1024 + swR1;
        const unsigned aTv0 = (16 * lw + r8) * 3072 + swT, aTv1 = (16 * lw + 8 + r8) * 3072 + swT;
        const char* bR = (tm < 16 ? wsre : wsim) + (size_t)(128 * (tm & 15)) * 4096 + (size_t)kh * 2048;
        const char* bT = xB + (size_t)(1024 * kh) * 1024 + (size_t)(128 * tn) * 2;
        const unsigned bRv0 = (32 * lw + r8) * 4096 + swR0, bRv1 = (32 * lw + 8 + r8) * 4096 + swR1;
        const unsigned bTv0 = (16 * lw + r8) * 1024 + swT, bTv1 = (16 * lw + 8 + r8) * 1024 + swT;

        auto issue = [&](int st) {
            const unsigned buf = (unsigned)(st & (NS - 1)) * STAGE_B;
            if (st < NT_A) {
                const char* R = aR + st * 128;
                const char* T = aT + (size_t)st * (64 * 3072);
                glds16(R + aRv0, buf + (2 * lw) * 1024);
                glds16(R + aRv1, buf + (2 * lw + 1) * 1024);
#pragma unroll
                for (int u = 0; u < 3; ++u) {
                    glds16(T + u * 1024 + aTv0, buf + 8192 + u * 8192 + (2 * lw) * 1024);
                    glds16(T + u * 1024 + aTv1, buf + 8192 + u * 8192 + (2 * lw + 1) * 1024);
                }
            } else {
                const int s2 = st - NT_A;
                const char* R = bR + s2 * 128;
                const char* T = bT + (size_t)s2 * (64 * 1024);
                glds16(R + bRv0, buf + (4 * lw) * 1024);
                glds16(R + bRv1, buf + (4 * lw + 1) * 1024);
                glds16(R + 16 * 4096 + bRv0, buf + (4 * lw + 2) * 1024);
                glds16(R + 16 * 4096 + bRv1, buf + (4 * lw + 3) * 1024);
#pragma unroll
                for (int u = 0; u < 2; ++u) {
                    glds16(T + u * 128 + bTv0, buf + 16384 + u * 8192 + (2 * lw) * 1024);
                    glds16(T + u * 128 + bTv1, buf + 16384 + u * 8192 + (2 * lw + 1) * 1024);
                }
            }
        };
#pragma unroll
        for (int i = 0; i < NS; ++i) issue(i);
        WAIT_VM(24);
        BARRIER();
        for (int i = 0; i < NT; ++i) {
            if (i <= NT - 4) { WAIT_VM(16); } else if (i == NT - 3) { WAIT_VM(8); } else { WAIT_VM(0); }
            BARRIER();
            if (i + NS < NT) issue(i + NS);
            if (i == NT_A - 1) BARRIER();
        }
        return;
    }

    const int h = l >> 5, l31 = l & 31;
    {
        const int f = b * 4 + w;
        const int ri = f >> 9, eb = (f >> 5) & 15, db = f & 31;
        const char* src = (ri ? wdim : wdre) + (size_t)(32 * eb + l31) * 1024 + (16 * db + 4 * h) * 2;
        u32x2 a = *(const u32x2*)src, c = *(const u32x2*)(src + 16);
        const unsigned sg = ri ? 0x80008000u : 0u;
        u32x4 v = {a[0] ^ sg, a[1] ^ sg, c[0] ^ sg, c[1] ^ sg};
        *(u32x4*)(ws + WS_WD + (size_t)f * 1024 + l * 16) = v;
        if (w < 2) {
            const int g = b * 2 + w;
            const int eb2 = g >> 5, hd = g & 31;
            const unsigned short* s2 = wo + (size_t)(16 * hd + 4 * h) * 512 + 32 * eb2 + l31;
            u32x4 o;
#pragma unroll
            for (int j = 0; j < 4; ++j) {
                const int d0 = ((2 * j) & 3) + 8 * ((2 * j) >> 2);
                o[j] = (unsigned)s2[d0 * 512] | ((unsigned)s2[(d0 + 1) * 512] << 16);
            }
            *(u32x4*)(ws + WS_WO + (size_t)g * 1024 + l * 16) = o;
        }
        if (w == 2 && l == 0) *(unsigned*)(ws + WS_FLAG + b * 4) = 0u;
    }
    const int sbk = w & 1, hp = w >> 1;
    u32x4 wf[2];
    {
        const int fr = (l31 & 3) + 4 * (l31 >> 3);
        const unsigned sg = ((l31 >> 2) & 1) ? 0x80008000u : 0u;
#pragma unroll
        for (int hh = 0; hh < 2; ++hh) {
            const unsigned short* s = wphi + (4 * hq + 2 * hp + hh) * 256 + (4 * h) * 16 + fr;
#pragma unroll
            for (int j = 0; j < 4; ++j) {
                const int d0 = ((2 * j) & 3) + 8 * ((2 * j) >> 2);
                wf[hh][j] = ((unsigned)s[d0 * 16] | ((unsigned)s[(d0 + 1) * 16] << 16)) ^ sg;
            }
        }
    }
    const unsigned swz = (unsigned)((l31 >> 1) & 7);
    const unsigned rlane = (unsigned)l31 * 128 + ((swz ^ (unsigned)h) << 4);
    const int g4 = l >> 4, q4 = (l >> 2) & 3, p4 = l & 3;
    const unsigned tl = (unsigned)(8 * (g4 >> 1) + q4) * 128 + (unsigned)(2 * (g4 & 1) + (p4 >> 1)) * 16 + 8 * (p4 & 1);
    const unsigned tlane0 = tl + ((unsigned)(q4 >> 1) << 6);
    const unsigned tlane1 = tl + ((unsigned)(1 ^ (q4 >> 1)) << 6);

    f32x16 aq = {}, ak = {}, av = {};
    {
        const unsigned rA = (unsigned)sbk * 4096 + rlane;
        const unsigned tA = 8192 + (hp ? tlane1 : tlane0);
        u32x4 x0, q0, k0, v0, x1, q1, k1, v1;
#define A_READ(X, Q, K, V, buf, ks) do { X = lds_r128((buf) + (rA ^ ((ks) << 5))); \
            Q = lds_trfrag((buf) + tA + (ks) * 2048); K = lds_trfrag((buf) + tA + 8192 + (ks) * 2048); \
            V = lds_trfrag((buf) + tA + 16384 + (ks) * 2048); } while (0)
#define A_MMA(X, Q, K, V) do { aq = mfma32(Q, X, aq); ak = mfma32(K, X, ak); av = mfma32(X, V, av); } while (0)
        BARRIER();
        A_READ(x0, q0, k0, v0, 0u, 0);
        for (int i = 0; i < NT_A; ++i) {
            const unsigned buf = (unsigned)(i & (NS - 1)) * STAGE_B, nbuf = (unsigned)((i + 1) & (NS - 1)) * STAGE_B;
            A_READ(x1, q1, k1, v1, buf, 1); A_MMA(x0, q0, k0, v0);
            A_READ(x0, q0, k0, v0, buf, 2); A_MMA(x1, q1, k1, v1);
            A_READ(x1, q1, k1, v1, buf, 3); A_MMA(x0, q0, k0, v0);
            WAIT_LGKM0_BARRIER();
            if (i + 1 < NT_A) A_READ(x0, q0, k0, v0, nbuf, 0);
            A_MMA(x1, q1, k1, v1);
        }
    }
    {
        const unsigned scr = NS * STAGE_B + (unsigned)w * 4096;
        const int sb = 2 * rb + sbk;
        f32x16 kvd[2];
#pragma unroll
        for (int hh = 0; hh < 2; ++hh) {
            const int head = 4 * hq + 2 * hp + hh;
            const f32x16 zero = {};
            u32x4 o0, o1;
            f32x16 pq = mfma32(wf[hh], pack8(aq, 8 * hh), zero);
            softmax16(pq, o0, o1);
            char* dst = ws + WS_PHIQ + ((size_t)(sb * 32 + head) * 2) * 1024 + l * 16;
            *(u32x4*)dst = o0; *(u32x4*)(dst + 1024) = o1;
            f32x16 pk = mfma32(wf[hh], pack8(ak, 8 * hh), zero);
            softmax16(pk, o0, o1);
            const unsigned wa = scr + hh * 2048 + l31 * 64 + h * 32;
            *(LDS_AS u32x4*)(uintptr_t)wa = o0; *(LDS_AS u32x4*)(uintptr_t)(wa + 16) = o1;
        }
        asm volatile("s_waitcnt lgkmcnt(0)" ::: "memory");
        const u32x4 vf0 = pack8(av, 0), vf1 = pack8(av, 8);
        const u32x4 ones = {0x3F803F80u, 0x3F803F80u, 0x3F803F80u, 0x3F803F80u};
#pragma unroll
        for (int hh = 0; hh < 2; ++hh) {
            const bool mine = (l31 >> 4) == hh;
            const u32x4 b0 = mine ? vf0 : ones, b1 = mine ? vf1 : ones;
            const unsigned ra = scr + hh * 2048 + (unsigned)(4 * (g4 >> 1) + q4) * 64 + (unsigned)(g4 & 1) * 32 + p4 * 8;
            u32x2 a0 = lds_tr64(ra), a1 = lds_tr64(ra + 512), a2 = lds_tr64(ra + 1024), a3 = lds_tr64(ra + 1536);
            u32x4 f0 = {a0[0], a0[1], a1[0], a1[1]}, f1 = {a2[0], a2[1], a3[0], a3[1]};
            f32x16 d = {};
            d = mfma32(f0, b0, d);
            d = mfma32(f1, b1, d);
            kvd[hh] = d;
        }
        const unsigned xch = NS * STAGE_B + 16384 + (unsigned)hp * 8192 + l * 16;
        if (sbk == 1) {
#pragma unroll
            for (int hh = 0; hh < 2; ++hh)
#pragma unroll
                for (int q = 0; q < 4; ++q) {
                    f32x4 v = {kvd[hh][4 * q], kvd[hh][4 * q + 1], kvd[hh][4 * q + 2], kvd[hh][4 * q + 3]};
                    *(LDS_AS f32x4*)(uintptr_t)(xch + (hh * 4 + q) * 1024) = v;
                }
        }
        WAIT_LGKM0_BARRIER();
        if (sbk == 0) {
#pragma unroll
            for (int hh = 0; hh < 2; ++hh) {
                const int head = 4 * hq + 2 * hp + hh;
                const int nl = (l31 - 16 * hh) & 31;
                char* dst = ws + WS_KVP + ((size_t)(rb * 32 + head) * 4) * 1024 + (32 * h + nl) * 16;
#pragma unroll
                for (int q = 0; q < 4; ++q) {
                    f32x4 v = *(const LDS_AS f32x4*)(uintptr_t)(xch + (hh * 4 + q) * 1024);
                    v[0] += kvd[hh][4 * q]; v[1] += kvd[hh][4 * q + 1]; v[2] += kvd[hh][4 * q + 2]; v[3] += kvd[hh][4 * q + 3];
                    if (nl <= 16) *(f32x4*)(dst + q * 1024) = v;
                }
            }
        }
    }
    {
        const int wr = w & 1, wc = w >> 1;
        const unsigned rB = (unsigned)wr * 8192 + rlane;
        const unsigned tB0 = 16384 + (unsigned)wc * 8192 + tlane0, tB1 = 16384 + (unsigned)wc * 8192 + tlane1;
        f32x16 c00 = {}, c01 = {}, c10 = {}, c11 = {};
        u32x4 r0a, r1a, t0a, t1a, r0b, r1b, t0b, t1b;
#define B_READ(R0, R1, T0, T1, buf, ks) do { R0 = lds_r128((buf) + (rB ^ ((ks) << 5))); R1 = lds_r128((buf) + 4096 + (rB ^ ((ks) << 5))); \
            T0 = lds_trfrag((buf) + tB0 + (ks) * 2048); T1 = lds_trfrag((buf) + tB1 + (ks) * 2048); } while (0)
#define B_MMA(R0, R1, T0, T1) do { c00 = mfma32(T0, R0, c00); c01 = mfma32(T0, R1, c01); c10 = mfma32(T1, R0, c10); c11 = mfma32(T1, R1, c11); } while (0)
        B_READ(r0a, r1a, t0a, t1a, (unsigned)(NT_A & (NS - 1)) * STAGE_B, 0);
        for (int i = NT_A; i < NT; ++i) {
            const unsigned buf = (unsigned)(i & (NS - 1)) * STAGE_B, nbuf = (unsigned)((i + 1) & (NS - 1)) * STAGE_B;
            B_READ(r0b, r1b, t0b, t1b, buf, 1); B_MMA(r0a, r1a, t0a, t1a);
            B_READ(r0a, r1a, t0a, t1a, buf, 2); B_MMA(r0b, r1b, t0b, t1b);
            B_READ(r0b, r1b, t0b, t1b, buf, 3); B_MMA(r0a, r1a, t0a, t1a);
            WAIT_LGKM0_BARRIER();
            if (i + 1 < NT) B_READ(r0a, r1a, t0a, t1a, nbuf, 0);
            B_MMA(r0b, r1b, t0b, t1b);
        }
        char* dst = ws + WS_PP + ((size_t)((kh * 128 + 4 * tm + 2 * wr) * 32 + 8 * tn + 4 * wc)) * 1024 + l * 16;
        *(u32x4*)(dst) = pack8(c00, 0);                 *(u32x4*)(dst + 1024) = pack8(c00, 8);
        *(u32x4*)(dst + 2048) = pack8(c10, 0);          *(u32x4*)(dst + 3072) = pack8(c10, 8);
        *(u32x4*)(dst + 32768) = pack8(c01, 0);         *(u32x4*)(dst + 32768 + 1024) = pack8(c01, 8);
        *(u32x4*)(dst + 32768 + 2048) = pack8(c11, 0);  *(u32x4*)(dst + 32768 + 3072) = pack8(c11, 8);
    }
}

constexpr int K2_ROW = 272;
constexpr int K2_PART = 64 * K2_ROW;
constexpr int K2_LDS = 8 * K2_PART;

__global__ __launch_bounds__(512, 2) void k2_kernel(char* __restrict__ ws, unsigned short* __restrict__ out)
{
    extern __shared__ __attribute__((aligned(16))) char shm[];
    (void)shm;
    const int tid = threadIdx.x, l = tid & 63;
    const int w = __builtin_amdgcn_readfirstlane(tid >> 6);
    const int b = blockIdx.x, xcd = b & 7, idx = b >> 3;
    const int tm = 4 * xcd + (idx >> 3), tn = idx & 7;
    const int h = l >> 5, l31 = l & 31;
    f32x16 c00 = {}, c01 = {}, c10 = {}, c11 = {};
    const auto rs = __builtin_amdgcn_make_buffer_rsrc((void*)ws, 0, (int)(WS_FLAG + 4096), 0x00020000);

    if (w < 4) {
        const int ri = w >> 1, dh = w & 1;
        const char* A = ws + WS_WD + ((size_t)((ri * 16 + 2 * tn) * 32 + 16 * dh)) * 1024 + l * 16;
        const char* P = ws + WS_PP + ((size_t)((ri * 64 + 2 * tm) * 32 + 16 * dh)) * 1024 + l * 16;
        constexpr int PD = 4;
        u32x4 fa[PD][2], fp[PD][4];
#define Y_LOAD(s, ks) do { fa[s][0] = *(const u32x4*)(A + (ks) * 1024); fa[s][1] = *(const u32x4*)(A + 32768 + (ks) * 1024); \
            fp[s][0] = *(const u32x4*)(P + (ks) * 1024); fp[s][1] = *(const u32x4*)(P + 32768 + (ks) * 1024); \
            fp[s][2] = *(const u32x4*)(P + (4 << 20) + (ks) * 1024); fp[s][3] = *(const u32x4*)(P + (4 << 20) + 32768 + (ks) * 1024); } while (0)
#pragma unroll
        for (int k = 0; k < PD; ++k) Y_LOAD(k, k);
#pragma unroll
        for (int k = 0; k < 16; ++k) {
            const int s = k % PD;
            c00 = mfma32(fa[s][0], fp[s][0], c00); c01 = mfma32(fa[s][0], fp[s][1], c01);
            c10 = mfma32(fa[s][1], fp[s][0], c10); c11 = mfma32(fa[s][1], fp[s][1], c11);
            c00 = mfma32(fa[s][0], fp[s][2], c00); c01 = mfma32(fa[s][0], fp[s][3], c01);
            c10 = mfma32(fa[s][1], fp[s][2], c10); c11 = mfma32(fa[s][1], fp[s][3], c11);
            if (k + PD < 16) Y_LOAD(s, k + PD);
        }
    } else {
        const int aw = w - 4;
        if (aw == 0) {
            const int head = b >> 3, q = (b >> 1) & 3, hq = b & 1;
            const char* src = ws + WS_KVP + ((size_t)(head * 4 + q)) * 1024 + (32 * hq + (l <= 16 ? l : 16)) * 16;
            f32x4 s = {0.f, 0.f, 0.f, 0.f};
            f32x4 v[32];
#pragma unroll
            for (int r = 0; r < 32; ++r) v[r] = *(const f32x4*)(src + (size_t)r * (32 * 4096));
#pragma unroll
            for (int r = 0; r < 32; ++r) s += v[r];
            const unsigned long long pv = (unsigned long long)pk2(s[0], s[1]) | ((unsigned long long)pk2(s[2], s[3]) << 32);
            if (l < 32) {
                unsigned long long* dst = (unsigned long long*)(ws + WS_KVZ + (size_t)(head * 2 + (q & 1)) * 1024 + (l + 32 * (q >> 1)) * 16 + 8 * hq);
                __hip_atomic_store(dst, pv, __ATOMIC_RELAXED, __HIP_MEMORY_SCOPE_AGENT);
            }
            asm volatile("s_waitcnt vmcnt(0)" ::: "memory");
            if (l == 0) __hip_atomic_store((unsigned*)(ws + WS_FLAG + b * 4), 1u, __ATOMIC_RELAXED, __HIP_MEMORY_SCOPE_AGENT);
        }
        const char* PH = ws + WS_PHIQ + ((size_t)((2 * tm) * 32 + 8 * aw) * 2) * 1024 + l * 16;
        const char* WO = ws + WS_WO + ((size_t)((2 * tn) * 32 + 8 * aw)) * 1024 + l * 16;
        constexpr int PD = 4;
        u32x4 fph[PD][4], fwo[PD][2], fkv[2][2];
#define O_LOAD(s, hd) do { fph[s][0] = *(const u32x4*)(PH + (hd) * 2048); fph[s][1] = *(const u32x4*)(PH + (hd) * 2048 + 1024); \
            fph[s][2] = *(const u32x4*)(PH + 65536 + (hd) * 2048); fph[s][3] = *(const u32x4*)(PH + 65536 + (hd) * 2048 + 1024); \
            fwo[s][0] = *(const u32x4*)(WO + (hd) * 1024); fwo[s][1] = *(const u32x4*)(WO + 32768 + (hd) * 1024); } while (0)
#pragma unroll
        for (int k = 0; k < PD; ++k) O_LOAD(k, k);
        {
            const unsigned* fl = (const unsigned*)(ws + WS_FLAG) + 64 * aw + l;
            for (int spin = 0; spin < (1 << 22); ++spin) {
                unsigned f = __hip_atomic_load(fl, __ATOMIC_RELAXED, __HIP_MEMORY_SCOPE_AGENT);
                if (__all(f == 1u)) break;
                __builtin_amdgcn_s_sleep(2);
            }
            asm volatile("" ::: "memory");
        }
        const unsigned kvo = (unsigned)WS_KVZ + (unsigned)(8 * aw) * 2048 + l * 16;
        fkv[0][0] = __builtin_amdgcn_raw_buffer_load_b128(rs, kvo, 0, 16);
        fkv[0][1] = __builtin_amdgcn_raw_buffer_load_b128(rs, kvo + 1024, 0, 16);
#pragma unroll
        for (int k = 0; k < 8; ++k) {
            const int s = k % PD, c = k & 1;
            if (k + 1 < 8) {
                fkv[c ^ 1][0] = __builtin_amdgcn_raw_buffer_load_b128(rs, kvo + (k + 1) * 2048, 0, 16);
                fkv[c ^ 1][1] = __builtin_amdgcn_raw_buffer_load_b128(rs, kvo + (k + 1) * 2048 + 1024, 0, 16);
            }
#pragma unroll
            for (int sbi = 0; sbi < 2; ++sbi) {
                f32x16 nd = {};
                nd = mfma32(fkv[c][0], fph[s][2 * sbi], nd);
                nd = mfma32(fkv[c][1], fph[s][2 * sbi + 1], nd);
                float den = bflo(pk2(nd[8], nd[8]) & 0xffffu);
                den = bflo(pk2(den + 1e-6f, 0.f) & 0xffffu);
                const float inv = __builtin_amdgcn_rcpf(den);
                u32x4 of;
#pragma unroll
                for (int j = 0; j < 4; ++j) {
                    const unsigned u = pk2(nd[2 * j], nd[2 * j + 1]);
                    of[j] = pk2(bflo(u) * inv, bfhi(u) * inv);
                }
                if (sbi == 0) { c00 = mfma32(fwo[s][0], of, c00); c10 = mfma32(fwo[s][1], of, c10); }
                else          { c01 = mfma32(fwo[s][0], of, c01); c11 = mfma32(fwo[s][1], of, c11); }
            }
            if (k + PD < 8) O_LOAD(s, k + PD);
        }
    }
    {
        const unsigned base = (unsigned)w * K2_PART + (unsigned)l31 * K2_ROW + h * 16;
#pragma unroll
        for (int q = 0; q < 4; ++q) {
            f32x4 v;
            v = (f32x4){c00[4 * q], c00[4 * q + 1], c00[4 * q + 2], c00[4 * q + 3]};
            *(LDS_AS f32x4*)(uintptr_t)(base + q * 32) = v;
            v = (f32x4){c10[4 * q], c10[4 * q + 1], c10[4 * q + 2], c10[4 * q + 3]};
            *(LDS_AS f32x4*)(uintptr_t)(base + 128 + q * 32) = v;
            v = (f32x4){c01[4 * q], c01[4 * q + 1], c01[4 * q + 2], c01[4 * q + 3]};
            *(LDS_AS f32x4*)(uintptr_t)(base + 32 * K2_ROW + q * 32) = v;
            v = (f32x4){c11[4 * q], c11[4 * q + 1], c11[4 * q + 2], c11[4 * q + 3]};
            *(LDS_AS f32x4*)(uintptr_t)(base + 32 * K2_ROW + 128 + q * 32) = v;
        }
        WAIT_LGKM0_BARRIER();
        const int grp = w >> 2, wq = w & 3;
#pragma unroll
        for (int ps = 0; ps < 2; ++ps) {
            const int row = 16 * wq + 8 * ps + (l >> 3), ch = l & 7;
            const unsigned ra = (unsigned)(grp * 4) * K2_PART + (unsigned)row * K2_ROW + ch * 32;
            f32x4 s0 = {0.f, 0.f, 0.f, 0.f}, s1 = {0.f, 0.f, 0.f, 0.f};
#pragma unroll
            for (int sw = 0; sw < 4; ++sw) {
                s0 += *(const LDS_AS f32x4*)(uintptr_t)(ra + sw * K2_PART);
                s1 += *(const LDS_AS f32x4*)(uintptr_t)(ra + sw * K2_PART + 16);
            }
            u32x4 o = {pk2(s0[0], s0[1]), pk2(s0[2], s0[3]), pk2(s1[0], s1[1]), pk2(s1[2], s1[3])};
            unsigned short* dst = out + (size_t)(64 * tm + row) * 1024 + (grp == 0 ? 512 : 0) + 64 * tn + ch * 8;
            *(u32x4*)dst = o;
        }
    }
}

extern "C" void kernel_launch(void* const* d_in, const int* in_sizes, int n_in,
                              void* d_out, int out_size, void* d_ws, size_t ws_size,
                              hipStream_t stream) {
    (void)in_sizes; (void)n_in; (void)out_size; (void)ws_size;
    (void)hipFuncSetAttribute((const void*)k1_kernel, hipFuncAttributeMaxDynamicSharedMemorySize, K1_LDS);
    (void)hipFuncSetAttribute((const void*)k2_kernel, hipFuncAttributeMaxDynamicSharedMemorySize, K2_LDS);
    k1_kernel<<<256, 512, K1_LDS, stream>>>(
        (const char*)d_in[0], (const char*)d_in[1], (const unsigned short*)d_in[2], (const unsigned short*)d_in[3],
        (const char*)d_in[4], (const char*)d_in[5], (const char*)d_in[6], (const char*)d_in[7], (const char*)d_in[8],
        (char*)d_ws);
    k2_kernel<<<256, 512, K2_LDS, stream>>>((char*)d_ws, (unsigned short*)d_out);
}
```

```cpp
#include <hip/hip_runtime.h>
#include <stdint.h>

typedef __bf16 bf16x8 __attribute__((ext_vector_type(8)));
typedef __bf16 bf16x2 __attribute__((ext_vector_type(2)));
typedef float f32x2 __attribute__((ext_vector_type(2)));
typedef float f32x4 __attribute__((ext_vector_type(4)));
typedef float f32x16 __attribute__((ext_vector_type(16)));
typedef unsigned u32x4 __attribute__((ext_vector_type(4)));
typedef unsigned u32x2 __attribute__((ext_vector_type(2)));
typedef short s16x4 __attribute__((ext_vector_type(4)));
#define LDS_AS __attribute__((address_space(3)))
#define GLB_AS __attribute__((address_space(1)))

constexpr size_t WS_PP   = 0;
constexpr size_t WS_PHIQ = (size_t)8 << 20;
constexpr size_t WS_KVP  = (size_t)12 << 20;
constexpr size_t WS_WD   = (size_t)16 << 20;
constexpr size_t WS_WO   = (size_t)17 << 20;
constexpr size_t WS_KVZ  = ((size_t)17 << 20) + ((size_t)512 << 10);
constexpr size_t WS_END  = (size_t)18 << 20;

constexpr int NS = 4;
constexpr int STAGE_B = 32768;
constexpr int K1_LDS = NS * STAGE_B + 32768;
constexpr int NT_A = 8, NT_B = 16, NT = NT_A + NT_B;

__device__ __forceinline__ unsigned pk2(float a, float b) {
    f32x2 v = {a, b};
    return __builtin_bit_cast(unsigned, __builtin_convertvector(v, bf16x2));
}
__device__ __forceinline__ float bflo(unsigned u) { return __builtin_bit_cast(float, u << 16); }
__device__ __forceinline__ float bfhi(unsigned u) { return __builtin_bit_cast(float, u & 0xffff0000u); }
__device__ __forceinline__ f32x16 mfma32(u32x4 a, u32x4 b, f32x16 c) {
    return __builtin_amdgcn_mfma_f32_32x32x16_bf16(__builtin_bit_cast(bf16x8, a), __builtin_bit_cast(bf16x8, b), c, 0, 0, 0);
}
__device__ __forceinline__ u32x4 lds_r128(unsigned addr) { return *(const LDS_AS u32x4*)(uintptr_t)addr; }
__device__ __forceinline__ u32x2 lds_tr64(unsigned addr) {
    s16x4 v = __builtin_amdgcn_ds_read_tr16_b64_v4i16((LDS_AS s16x4*)(uintptr_t)addr);
    return __builtin_bit_cast(u32x2, v);
}
__device__ __forceinline__ u32x4 lds_trfrag(unsigned addr) {
    u32x2 lo = lds_tr64(addr), hi = lds_tr64(addr + 512);
    u32x4 r = {lo[0], lo[1], hi[0], hi[1]};
    return r;
}
__device__ __forceinline__ void glds16(const char* g, unsigned lds) {
    __builtin_amdgcn_global_load_lds((const GLB_AS void*)g, (LDS_AS void*)(uintptr_t)lds, 16, 0, 0);
}
#define STAMP(k, i) do {} while (0)
#define BARRIER() asm volatile("s_barrier" ::: "memory")
#define WAIT_LGKM0_BARRIER() asm volatile("s_waitcnt lgkmcnt(0)\n\ts_barrier" ::: "memory")
#define WAIT_VM(n) asm volatile("s_waitcnt vmcnt(" #n ")" ::: "memory")

#define SB() __builtin_amdgcn_sched_barrier(0)

__device__ __forceinline__ void softmax16(const f32x16& p, u32x4& o0, u32x4& o1) {
    float x[16];
#pragma unroll
    for (int i = 0; i < 8; ++i) { unsigned u = pk2(p[2 * i], p[2 * i + 1]); x[2 * i] = bflo(u); x[2 * i + 1] = bfhi(u); }
    float m = fmaxf(x[0], x[1]);
#pragma unroll
    for (int i = 1; i < 8; ++i) m = fmaxf(fmaxf(m, x[2 * i]), x[2 * i + 1]);
    const float L2E = 1.4426950408889634f;
    const float mb = -m * L2E;
    float s0 = 0.f, s1 = 0.f;
#pragma unroll
    for (int i = 0; i < 8; ++i) {
        x[2 * i] = __builtin_amdgcn_exp2f(__builtin_fmaf(x[2 * i], L2E, mb)); s0 += x[2 * i];
        x[2 * i + 1] = __builtin_amdgcn_exp2f(__builtin_fmaf(x[2 * i + 1], L2E, mb)); s1 += x[2 * i + 1];
    }
    const float s = s0 + s1;
    float inv = __builtin_amdgcn_rcpf(s);
#pragma unroll
    for (int i = 0; i < 4; ++i) { o0[i] = pk2(x[2 * i] * inv, x[2 * i + 1] * inv); o1[i] = pk2(x[8 + 2 * i] * inv, x[9 + 2 * i] * inv); }
}
__device__ __forceinline__ u32x4 pack8(const f32x16& a, int o) {
    u32x4 r;
#pragma unroll
    for (int i = 0; i < 4; ++i) r[i] = pk2(a[o + 2 * i], a[o + 2 * i + 1]);
    return r;
}

__global__ __launch_bounds__(512, 2) void k1_kernel(
    const char* __restrict__ xA, const char* __restrict__ wqkv, const unsigned short* __restrict__ wphi,
    const unsigned short* __restrict__ wo, const char* __restrict__ xB, const char* __restrict__ wsre,
    const char* __restrict__ wsim, const char* __restrict__ wdre, const char* __restrict__ wdim, char* __restrict__ ws)
{
    extern __shared__ __attribute__((aligned(16))) char shm[];
    (void)shm;
    const int tid = threadIdx.x, l = tid & 63;
    const int w = __builtin_amdgcn_readfirstlane(tid >> 6);
    const int b = blockIdx.x, xcd = b & 7, idx = b >> 3;
    const int tsel = idx >> 3;
    const int tm = (tsel >> 1) * 16 + 2 * xcd + (tsel & 1);
    const int tn = (idx >> 1) & 3, kh = idx & 1;
    const int rb = 4 * xcd + (idx >> 3), hq = idx & 7;
    const int h = l >> 5, l31 = l & 31;
    const int g4 = l >> 4, q4 = (l >> 2) & 3, p4 = l & 3;
    constexpr unsigned SCR = NS * STAGE_B;

    if (w >= 4) {
        const int lw = w - 4;
        STAMP(0, 0);
        const unsigned r8 = l >> 3, swT = (unsigned)(((l & 7) ^ (((l >> 4) & 1) << 2)) << 4);
        const unsigned swR0 = (unsigned)(((l & 7) ^ (l >> 4)) << 4), swR1 = (unsigned)(((l & 7) ^ ((l >> 4) + 4)) << 4);
        const char* aR = xA + (size_t)(64 * rb) * 1024;
        const char* aT = wqkv + (size_t)(64 * hq) * 2;
        const unsigned aRv0 = (16 * lw + r8) * 1024 + swR0, aRv1 = (16 * lw + 8 + r8) * 1024 + swR1;
        const unsigned aTv0 = (16 * lw + r8) * 3072 + swT, aTv1 = (16 * lw + 8 + r8) * 3072 + swT;
        const char* bR = (tm < 16 ? wsre : wsim) + (size_t)(128 * (tm & 15)) * 4096 + (size_t)kh * 2048;
        const char* bT = xB + (size_t)(1024 * kh) * 1024 + (size_t)(128 * tn) * 2;
        const unsigned bRv0 = (32 * lw + r8) * 4096 + swR0, bRv1 = (32 * lw + 8 + r8) * 4096 + swR1;
        const unsigned bTv0 = (16 * lw + r8) * 1024 + swT, bTv1 = (16 * lw + 8 + r8) * 1024 + swT;

        auto issueA = [&](int st) {
            const unsigned buf = (unsigned)(st & (NS - 1)) * STAGE_B;
            const char* R = aR + st * 128;
            const char* T = aT + (size_t)st * (64 * 3072);
            glds16(R + aRv0, buf + (2 * lw) * 1024);
            glds16(R + aRv1, buf + (2 * lw + 1) * 1024);
#pragma unroll
            for (int u = 0; u < 3; ++u) {
                glds16(T + u * 1024 + aTv0, buf + 8192 + u * 8192 + (2 * lw) * 1024);
                glds16(T + u * 1024 + aTv1, buf + 8192 + u * 8192 + (2 * lw + 1) * 1024);
            }
        };
        auto issueB = [&](int s2) {
            const unsigned buf = (unsigned)((s2 + NT_A) & (NS - 1)) * STAGE_B;
            const char* R = bR + s2 * 128;
            const char* T = bT + (size_t)s2 * (64 * 1024);
            glds16(R + bRv0, buf + (4 * lw) * 1024);
            glds16(R + bRv1, buf + (4 * lw + 1) * 1024);
            glds16(R + 16 * 4096 + bRv0, buf + (4 * lw + 2) * 1024);
            glds16(R + 16 * 4096 + bRv1, buf + (4 * lw + 3) * 1024);
#pragma unroll
            for (int u = 0; u < 2; ++u) {
                glds16(T + u * 128 + bTv0, buf + 16384 + u * 8192 + (2 * lw) * 1024);
                glds16(T + u * 128 + bTv1, buf + 16384 + u * 8192 + (2 * lw + 1) * 1024);
            }
        };
        issueA(0); issueA(1);
        WAIT_VM(8);
        BARRIER();
        issueA(2); issueA(3);
        STAMP(0, 1);
        for (int i = 0; i < NT_A; ++i) {
            WAIT_VM(16);
            BARRIER();
            if (i + NS < NT_A) issueA(i + NS); else issueB(i + NS - NT_A);
        }
        BARRIER();
        STAMP(0, 3);
        const unsigned scr = SCR + (unsigned)lw * 8192;
        f32x16 kvd[2];
        {
            const u32x4 vf0 = lds_r128(scr + 2048 + l * 16), vf1 = lds_r128(scr + 3072 + l * 16);
            const u32x4 ones = {0x3F803F80u, 0x3F803F80u, 0x3F803F80u, 0x3F803F80u};
            const f32x16 zero = {};
#pragma unroll
            for (int hh = 0; hh < 2; ++hh) {
                const u32x4 kf = lds_r128(scr + hh * 1024 + l * 16), wf = lds_r128(scr + 4096 + hh * 1024 + l * 16);
                u32x4 o0, o1;
                f32x16 pk = mfma32(wf, kf, zero);
                softmax16(pk, o0, o1);
                const unsigned wa = scr + 6144 + l31 * 64 + h * 32;
                *(LDS_AS u32x4*)(uintptr_t)wa = o0; *(LDS_AS u32x4*)(uintptr_t)(wa + 16) = o1;
                asm volatile("s_waitcnt lgkmcnt(0)" ::: "memory");
                const bool mine = (l31 >> 4) == hh;
                const u32x4 b0 = mine ? vf0 : ones, b1 = mine ? vf1 : ones;
                const unsigned ra = scr + 6144 + (unsigned)(4 * (g4 >> 1) + q4) * 64 + (unsigned)(g4 & 1) * 32 + p4 * 8;
                u32x2 a0 = lds_tr64(ra), a1 = lds_tr64(ra + 512), a2 = lds_tr64(ra + 1024), a3 = lds_tr64(ra + 1536);
                u32x4 f0 = {a0[0], a0[1], a1[0], a1[1]}, f1 = {a2[0], a2[1], a3[0], a3[1]};
                f32x16 d = mfma32(f0, b0, zero);
                d = mfma32(f1, b1, d);
                kvd[hh] = d;
                asm volatile("s_waitcnt lgkmcnt(0)" ::: "memory");
            }
        }
        if (lw & 1) {
#pragma unroll
            for (int hh = 0; hh < 2; ++hh)
#pragma unroll
                for (int q = 0; q < 4; ++q) {
                    f32x4 v = {kvd[hh][4 * q], kvd[hh][4 * q + 1], kvd[hh][4 * q + 2], kvd[hh][4 * q + 3]};
                    *(LDS_AS f32x4*)(uintptr_t)(scr + (hh * 4 + q) * 1024 + l * 16) = v;
                }
        }
        STAMP(0, 4);
        for (int i = NT_A; i < NT; ++i) {
            if (i <= NT - 4) { WAIT_VM(16); } else if (i == NT - 3) { WAIT_VM(8); } else { WAIT_VM(0); }
            WAIT_LGKM0_BARRIER();
            if (i + NS < NT) issueB(i + NS - NT_A);
            if (i == NT_A && !(lw & 1)) {
#pragma unroll
                for (int hh = 0; hh < 2; ++hh) {
                    const int head = 4 * hq + (lw & 2) + hh;
                    const int nl = (l31 - 16 * hh) & 31;
                    char* dst = ws + WS_KVP + ((size_t)(rb * 32 + head) * 4) * 1024 + (32 * h + nl) * 16;
#pragma unroll
                    for (int q = 0; q < 4; ++q) {
                        f32x4 v = *(const LDS_AS f32x4*)(uintptr_t)(scr + 8192 + (hh * 4 + q) * 1024 + l * 16);
                        v[0] += kvd[hh][4 * q]; v[1] += kvd[hh][4 * q + 1]; v[2] += kvd[hh][4 * q + 2]; v[3] += kvd[hh][4 * q + 3];
                        if (nl <= 16) *(f32x4*)(dst + q * 1024) = v;
                    }
                }
            }
        }
        STAMP(0, 2);
        return;
    }

    STAMP(0, 0);
    {
        const int f = b * 4 + w;
        const int ri = f >> 9, eb = (f >> 5) & 15, db = f & 31;
        const char* src = (ri ? wdim : wdre) + (size_t)(32 * eb + l31) * 1024 + (16 * db + 4 * h) * 2;
        u32x2 a = *(const u32x2*)src, c = *(const u32x2*)(src + 16);
        const unsigned sg = ri ? 0x80008000u : 0u;
        u32x4 v = {a[0] ^ sg, a[1] ^ sg, c[0] ^ sg, c[1] ^ sg};
        *(u32x4*)(ws + WS_WD + (size_t)f * 1024 + l * 16) = v;
        if (w < 2) {
            const int g = b * 2 + w;
            const int eb2 = g >> 5, hd = g & 31;
            const unsigned short* s2 = wo + (size_t)(16 * hd + 4 * h) * 512 + 32 * eb2 + l31;
            u32x4 o;
#pragma unroll
            for (int j = 0; j < 4; ++j) {
                const int d0 = ((2 * j) & 3) + 8 * ((2 * j) >> 2);
                o[j] = (unsigned)s2[d0 * 512] | ((unsigned)s2[(d0 + 1) * 512] << 16);
            }
            *(u32x4*)(ws + WS_WO + (size_t)g * 1024 + l * 16) = o;
        }
        if (w == 2 && l < 16) {
            const u32x4 poison = {0xFFFFFFFFu, 0xFFFFFFFFu, 0xFFFFFFFFu, 0xFFFFFFFFu};
            *(u32x4*)(ws + WS_KVZ + b * 256 + l * 16) = poison;
        }
    }
    const int sbk = w & 1, hp = w >> 1;
    u32x4 wf[2];
    {
        const int fr = (l31 & 3) + 4 * (l31 >> 3);
        const unsigned sg = ((l31 >> 2) & 1) ? 0x80008000u : 0u;
#pragma unroll
        for (int hh = 0; hh < 2; ++hh) {
            const unsigned short* s = wphi + (4 * hq + 2 * hp + hh) * 256 + (4 * h) * 16 + fr;
#pragma unroll
            for (int j = 0; j < 4; ++j) {
                const int d0 = ((2 * j) & 3) + 8 * ((2 * j) >> 2);
                wf[hh][j] = ((unsigned)s[d0 * 16] | ((unsigned)s[(d0 + 1) * 16] << 16)) ^ sg;
            }
        }
    }
    const unsigned swz = (unsigned)((l31 >> 1) & 7);
    const unsigned rlane = (unsigned)l31 * 128 + ((swz ^ (unsigned)h) << 4);
    const unsigned tl = (unsigned)(8 * (g4 >> 1) + q4) * 128 + (unsigned)(2 * (g4 & 1) + (p4 >> 1)) * 16 + 8 * (p4 & 1);
    const unsigned tlane0 = tl + ((unsigned)(q4 >> 1) << 6);
    const unsigned tlane1 = tl + ((unsigned)(1 ^ (q4 >> 1)) << 6);

    f32x16 aq = {}, ak = {}, av = {};
    {
        const unsigned rA = (unsigned)sbk * 4096 + rlane;
        const unsigned tA = 8192 + (hp ? tlane1 : tlane0);
        u32x4 x0, q0, k0, v0, x1, q1, k1, v1;
#define A_STEP(X, Q, K, V, nX, nQ, nK, nV, ra, ta) do { \
            aq = mfma32(Q, X, aq); SB(); \
            nX = lds_r128(ra); nQ = lds_trfrag(ta); nK = lds_trfrag((ta) + 8192); SB(); \
            ak = mfma32(K, X, ak); SB(); \
            nV = lds_trfrag((ta) + 16384); SB(); \
            av = mfma32(X, V, av); SB(); } while (0)
        STAMP(0, 1);
        BARRIER();
        STAMP(0, 2);
        x0 = lds_r128(rA); q0 = lds_trfrag(tA); k0 = lds_trfrag(tA + 8192); v0 = lds_trfrag(tA + 16384);
        SB();
        for (int i = 0; i < NT_A; ++i) {
            const unsigned buf = (unsigned)(i & (NS - 1)) * STAGE_B, nbuf = (unsigned)((i + 1) & (NS - 1)) * STAGE_B;
            const unsigned ra = buf + rA, ta = buf + tA;
            A_STEP(x0, q0, k0, v0, x1, q1, k1, v1, buf + (rA ^ 32), ta + 2048);
            A_STEP(x1, q1, k1, v1, x0, q0, k0, v0, buf + (rA ^ 64), ta + 4096);
            A_STEP(x0, q0, k0, v0, x1, q1, k1, v1, buf + (rA ^ 96), ta + 6144);
            (void)ra;
            WAIT_LGKM0_BARRIER();
            SB();
            A_STEP(x1, q1, k1, v1, x0, q0, k0, v0, nbuf + rA, nbuf + tA);
        }
    }
    STAMP(0, 3);
    {
        const unsigned scr = SCR + (unsigned)w * 8192 + l * 16;
        *(LDS_AS u32x4*)(uintptr_t)(scr) = pack8(ak, 0);
        *(LDS_AS u32x4*)(uintptr_t)(scr + 1024) = pack8(ak, 8);
        *(LDS_AS u32x4*)(uintptr_t)(scr + 2048) = pack8(av, 0);
        *(LDS_AS u32x4*)(uintptr_t)(scr + 3072) = pack8(av, 8);
        *(LDS_AS u32x4*)(uintptr_t)(scr + 4096) = wf[0];
        *(LDS_AS u32x4*)(uintptr_t)(scr + 5120) = wf[1];
        WAIT_LGKM0_BARRIER();
        const int sb = 2 * rb + sbk;
#pragma unroll
        for (int hh = 0; hh < 2; ++hh) {
            const int head = 4 * hq + 2 * hp + hh;
            const f32x16 zero = {};
            u32x4 o0, o1;
            f32x16 pq = mfma32(wf[hh], pack8(aq, 8 * hh), zero);
            softmax16(pq, o0, o1);
            char* dst = ws + WS_PHIQ + ((size_t)(sb * 32 + head) * 2) * 1024 + l * 16;
            *(u32x4*)dst = o0; *(u32x4*)(dst + 1024) = o1;
        }
    }
    STAMP(0, 4);
    {
        const int wr = w & 1, wc = w >> 1;
        const unsigned rB = (unsigned)wr * 8192 + rlane;
        const unsigned tB0 = 16384 + (unsigned)wc * 8192 + tlane0, tB1 = 16384 + (unsigned)wc * 8192 + tlane1;
        f32x16 c00 = {}, c01 = {}, c10 = {}, c11 = {};
        u32x4 r0a, r1a, t0a, t1a, r0b, r1b, t0b, t1b;
#define B_STEP(R0, R1, T0, T1, nR0, nR1, nT0, nT1, ra, tb) do { \
            c00 = mfma32(T0, R0, c00); SB(); \
            nR0 = lds_r128(ra); nT0 = lds_trfrag((tb) + tB0); SB(); \
            c01 = mfma32(T0, R1, c01); SB(); \
            nR1 = lds_r128((ra) + 4096); nT1 = lds_trfrag((tb) + tB1); SB(); \
            c10 = mfma32(T1, R0, c10); SB(); \
            c11 = mfma32(T1, R1, c11); SB(); } while (0)
        {
            const unsigned buf = (unsigned)(NT_A & (NS - 1)) * STAGE_B;
            r0a = lds_r128(buf + rB); r1a = lds_r128(buf + 4096 + rB); t0a = lds_trfrag(buf + tB0); t1a = lds_trfrag(buf + tB1);
            SB();
        }
        for (int i = NT_A; i < NT; ++i) {
            const unsigned buf = (unsigned)(i & (NS - 1)) * STAGE_B, nbuf = (unsigned)((i + 1) & (NS - 1)) * STAGE_B;
            B_STEP(r0a, r1a, t0a, t1a, r0b, r1b, t0b, t1b, buf + (rB ^ 32), buf + 2048);
            B_STEP(r0b, r1b, t0b, t1b, r0a, r1a, t0a, t1a, buf + (rB ^ 64), buf + 4096);
            B_STEP(r0a, r1a, t0a, t1a, r0b, r1b, t0b, t1b, buf + (rB ^ 96), buf + 6144);
            WAIT_LGKM0_BARRIER();
            SB();
            B_STEP(r0b, r1b, t0b, t1b, r0a, r1a, t0a, t1a, nbuf + rB, nbuf);
        }
        STAMP(0, 5);
        char* dst = ws + WS_PP + ((size_t)((kh * 128 + 4 * tm + 2 * wr) * 32 + 8 * tn + 4 * wc)) * 1024 + l * 16;
        *(u32x4*)(dst) = pack8(c00, 0);                 *(u32x4*)(dst + 1024) = pack8(c00, 8);
        *(u32x4*)(dst + 2048) = pack8(c10, 0);          *(u32x4*)(dst + 3072) = pack8(c10, 8);
        *(u32x4*)(dst + 32768) = pack8(c01, 0);         *(u32x4*)(dst + 32768 + 1024) = pack8(c01, 8);
        *(u32x4*)(dst + 32768 + 2048) = pack8(c11, 0);  *(u32x4*)(dst + 32768 + 3072) = pack8(c11, 8);
        STAMP(0, 6);
    }
}

constexpr int K2_LDS = NS * STAGE_B;

__device__ __forceinline__ void store_tile_T(const f32x16& c, unsigned short* rowp  , int h) {
#pragma unroll
    for (int qp = 0; qp < 2; ++qp) {
        unsigned a0 = pk2(c[8 * qp], c[8 * qp + 1]), a1 = pk2(c[8 * qp + 2], c[8 * qp + 3]);
        unsigned b0 = pk2(c[8 * qp + 4], c[8 * qp + 5]), b1 = pk2(c[8 * qp + 6], c[8 * qp + 7]);
        auto s0 = __builtin_amdgcn_permlane32_swap(a0, b0, false, false);
        auto s1 = __builtin_amdgcn_permlane32_swap(a1, b1, false, false);
        u32x4 o = {s0[0], s1[0], s0[1], s1[1]};
        *(u32x4*)(rowp + 16 * qp + 8 * h) = o;
    }
}

__global__ __launch_bounds__(512, 2) void k2_kernel(char* __restrict__ ws, unsigned short* __restrict__ out)
{
    extern __shared__ __attribute__((aligned(16))) char shm[];
    (void)shm;
    const int tid = threadIdx.x, l = tid & 63;
    const int w = __builtin_amdgcn_readfirstlane(tid >> 6);
    const int b = blockIdx.x, xcd = b & 7, idx = b >> 3;
    const int role = idx & 1, j16 = idx >> 1;
    const int tm = 4 * xcd + (j16 >> 2), tn = j16 & 3;
    const int h = l >> 5, l31 = l & 31;
    STAMP(1, 0);
    if (role == 0) {
        if (w >= 4) {
            const int lw = w - 4;
            const char* s0; const char* s1; size_t rstep;
            if (lw < 2) {
                s0 = ws + WS_PP + ((size_t)((lw * 128 + 2 * tm) * 32)) * 1024 + l * 16; s1 = s0 + 32768; rstep = (size_t)64 * 32768;
            } else {
                s0 = ws + WS_WD + ((size_t)((4 * tn + 2 * (lw - 2)) * 32)) * 1024 + l * 16; s1 = s0 + 32768; rstep = (size_t)16 * 32768;
            }
            auto issue = [&](int st) {
                const unsigned buf = (unsigned)(st & (NS - 1)) * STAGE_B + (unsigned)lw * 8192;
                const size_t o = (size_t)(st >> 3) * rstep + (size_t)(st & 7) * 4096;
#pragma unroll
                for (int k = 0; k < 4; ++k) glds16(s0 + o + k * 1024, buf + k * 1024);
#pragma unroll
                for (int k = 0; k < 4; ++k) glds16(s1 + o + k * 1024, buf + 4096 + k * 1024);
            };
            issue(0); issue(1);
            WAIT_VM(8);
            BARRIER();
            issue(2); issue(3);
            for (int i = 0; i < 16; ++i) {
                if (i <= 12) { WAIT_VM(16); } else if (i == 13) { WAIT_VM(8); } else { WAIT_VM(0); }
                BARRIER();
                if (i + NS < 16) issue(i + NS);
            }
            STAMP(1, 3);
            return;
        }
        const int tb = w & 1, eh = w >> 1;
        const unsigned pP0 = (unsigned)tb * 4096 + l * 16, pP1 = 8192 + (unsigned)tb * 4096 + l * 16;
        const unsigned pW0 = 16384 + (unsigned)eh * 8192 + l * 16, pW1 = pW0 + 4096;
        f32x16 c0 = {}, c1 = {};
        u32x4 p0a, p1a, w0a, w1a, p0b, p1b, w0b, w1b;
#define Y_STEP(P0, P1, W0, W1, nP0, nP1, nW0, nW1, o) do { \
            c0 = mfma32(W0, P0, c0); SB(); \
            nP0 = lds_r128((o) + pP0); nW0 = lds_r128((o) + pW0); SB(); \
            c1 = mfma32(W1, P0, c1); SB(); \
            nP1 = lds_r128((o) + pP1); nW1 = lds_r128((o) + pW1); SB(); \
            c0 = mfma32(W0, P1, c0); SB(); \
            c1 = mfma32(W1, P1, c1); SB(); } while (0)
        BARRIER();
        STAMP(1, 1);
        p0a = lds_r128(pP0); p1a = lds_r128(pP1); w0a = lds_r128(pW0); w1a = lds_r128(pW1);
        SB();
        for (int i = 0; i < 16; ++i) {
            const unsigned buf = (unsigned)(i & (NS - 1)) * STAGE_B, nbuf = (unsigned)((i + 1) & (NS - 1)) * STAGE_B;
            Y_STEP(p0a, p1a, w0a, w1a, p0b, p1b, w0b, w1b, buf + 1024);
            Y_STEP(p0b, p1b, w0b, w1b, p0a, p1a, w0a, w1a, buf + 2048);
            Y_STEP(p0a, p1a, w0a, w1a, p0b, p1b, w0b, w1b, buf + 3072);
            WAIT_LGKM0_BARRIER();
            SB();
            Y_STEP(p0b, p1b, w0b, w1b, p0a, p1a, w0a, w1a, nbuf);
        }
        STAMP(1, 3);
        unsigned short* rowp = out + (size_t)(64 * tm + 32 * tb + l31) * 1024 + 512 + 128 * tn + 64 * eh;
        store_tile_T(c0, rowp, h);
        store_tile_T(c1, rowp + 32, h);
        STAMP(1, 5);
        return;
    }
    {
        if (w < 2) {
            const int sl = (xcd * 16 + j16) * 2 + w;
            const int head = sl >> 3, q = (sl >> 1) & 3, hq = sl & 1;
            const char* src = ws + WS_KVP + ((size_t)(head * 4 + q)) * 1024 + (32 * hq + (l <= 16 ? l : 16)) * 16;
            f32x4 v[32];
#pragma unroll
            for (int r = 0; r < 32; ++r) v[r] = *(const f32x4*)(src + (size_t)r * (32 * 4096));
            f32x4 s0 = {0.f, 0.f, 0.f, 0.f}, s1 = {0.f, 0.f, 0.f, 0.f};
#pragma unroll
            for (int r = 0; r < 16; ++r) { s0 += v[2 * r]; s1 += v[2 * r + 1]; }
            s0 += s1;
            const unsigned long long pv = (unsigned long long)pk2(s0[0], s0[1]) | ((unsigned long long)pk2(s0[2], s0[3]) << 32);
            if (l < 32) {
                unsigned long long* dst = (unsigned long long*)(ws + WS_KVZ + (size_t)(head * 2 + (q & 1)) * 1024 + (l + 32 * (q >> 1)) * 16 + 8 * hq);
                __hip_atomic_store(dst, pv, __ATOMIC_RELAXED, __HIP_MEMORY_SCOPE_AGENT);
            }
            SB();
        }
        STAMP(1, 1);
        u32x4 fph[4][4], fwo[16], fkv[4][2];
        {
            const char* PH = ws + WS_PHIQ + ((size_t)((2 * tm) * 32 + 4 * w) * 2) * 1024 + l * 16;
#pragma unroll
            for (int k = 0; k < 4; ++k) {
                fph[k][0] = *(const u32x4*)(PH + k * 2048); fph[k][1] = *(const u32x4*)(PH + k * 2048 + 1024);
                fph[k][2] = *(const u32x4*)(PH + 65536 + k * 2048); fph[k][3] = *(const u32x4*)(PH + 65536 + k * 2048 + 1024);
            }
            const char* WO = ws + WS_WO + ((size_t)((4 * tn + (w & 3)) * 32 + 16 * (w >> 2))) * 1024 + l * 16;
#pragma unroll
            for (int k = 0; k < 16; ++k) fwo[k] = *(const u32x4*)(WO + k * 1024);
            SB();
        }
        {
            const auto rs = __builtin_amdgcn_make_buffer_rsrc((void*)(ws + WS_KVZ), 0, 65536, 0x00020000);
            const unsigned kvo = (unsigned)(4 * w) * 2048 + l * 16;
            asm volatile("s_waitcnt vmcnt(0)" ::: "memory");
            for (int spin = 0; spin < (1 << 16); ++spin) {
                bool ok = true;
#pragma unroll
                for (int k = 0; k < 4; ++k) {
                    fkv[k][0] = __builtin_amdgcn_raw_buffer_load_b128(rs, kvo + k * 2048, 0, 16);
                    fkv[k][1] = __builtin_amdgcn_raw_buffer_load_b128(rs, kvo + k * 2048 + 1024, 0, 16);
                }
#pragma unroll
                for (int k = 0; k < 4; ++k)
#pragma unroll
                    for (int i = 0; i < 4; ++i) ok = ok && fkv[k][0][i] != 0xFFFFFFFFu && fkv[k][1][i] != 0xFFFFFFFFu;
                if (__all(ok)) break;
                __builtin_amdgcn_s_sleep(8);
            }
        }
        STAMP(1, 2);
#pragma unroll
        for (int k = 0; k < 4; ++k) {
#pragma unroll
            for (int sbi = 0; sbi < 2; ++sbi) {
                f32x16 nd = {};
                nd = mfma32(fkv[k][0], fph[k][2 * sbi], nd);
                nd = mfma32(fkv[k][1], fph[k][2 * sbi + 1], nd);
                float den = bflo(pk2(nd[8], nd[8]) & 0xffffu);
                den = bflo(pk2(den + 1e-6f, 0.f) & 0xffffu);
                const float inv = __builtin_amdgcn_rcpf(den);
                u32x4 of;
#pragma unroll
                for (int jj = 0; jj < 4; ++jj) {
                    const unsigned u = pk2(nd[2 * jj], nd[2 * jj + 1]);
                    of[jj] = pk2(bflo(u) * inv, bfhi(u) * inv);
                }
                *(LDS_AS u32x4*)(uintptr_t)((unsigned)(sbi * 32 + 4 * w + k) * 1024 + l * 16) = of;
            }
        }
        WAIT_LGKM0_BARRIER();
        STAMP(1, 3);
        f32x16 c0 = {}, c1 = {};
        {
            const unsigned ob = (unsigned)(16 * (w >> 2)) * 1024 + l * 16;
#pragma unroll
            for (int k = 0; k < 16; ++k) {
                const u32x4 o0 = lds_r128(ob + k * 1024), o1 = lds_r128(ob + 32768 + k * 1024);
                c0 = mfma32(fwo[k], o0, c0);
                c1 = mfma32(fwo[k], o1, c1);
            }
        }
        STAMP(1, 4);
        const unsigned xb = 65536 + (unsigned)(w & 3) * 8192 + l * 16;
        if (w >= 4) {
#pragma unroll
            for (int q = 0; q < 4; ++q) {
                *(LDS_AS f32x4*)(uintptr_t)(xb + q * 1024) = (f32x4){c0[4 * q], c0[4 * q + 1], c0[4 * q + 2], c0[4 * q + 3]};
                *(LDS_AS f32x4*)(uintptr_t)(xb + 4096 + q * 1024) = (f32x4){c1[4 * q], c1[4 * q + 1], c1[4 * q + 2], c1[4 * q + 3]};
            }
        }
        WAIT_LGKM0_BARRIER();
        if (w < 4) {
#pragma unroll
            for (int q = 0; q < 4; ++q) {
                const f32x4 a = *(const LDS_AS f32x4*)(uintptr_t)(xb + q * 1024);
                const f32x4 c = *(const LDS_AS f32x4*)(uintptr_t)(xb + 4096 + q * 1024);
#pragma unroll
                for (int i = 0; i < 4; ++i) { c0[4 * q + i] += a[i]; c1[4 * q + i] += c[i]; }
            }
            unsigned short* rowp = out + (size_t)(64 * tm + l31) * 1024 + 128 * tn + 32 * w;
            store_tile_T(c0, rowp, h);
            store_tile_T(c1, rowp + 32 * 1024, h);
        }
        STAMP(1, 5);
    }
}

extern "C" void kernel_launch(void* const* d_in, const int* in_sizes, int n_in,
                              void* d_out, int out_size, void* d_ws, size_t ws_size,
                              hipStream_t stream) {
    (void)in_sizes; (void)n_in; (void)out_size; (void)ws_size;
    (void)hipFuncSetAttribute((const void*)k1_kernel, hipFuncAttributeMaxDynamicSharedMemorySize, K1_LDS);
    (void)hipFuncSetAttribute((const void*)k2_kernel, hipFuncAttributeMaxDynamicSharedMemorySize, K2_LDS);
    k1_kernel<<<256, 512, K1_LDS, stream>>>(
        (const char*)d_in[0], (const char*)d_in[1], (const unsigned short*)d_in[2], (const unsigned short*)d_in[3],
        (const char*)d_in[4], (const char*)d_in[5], (const char*)d_in[6], (const char*)d_in[7], (const char*)d_in[8],
        (char*)d_ws);
    k2_kernel<<<256, 512, K2_LDS, stream>>>((char*)d_ws, (unsigned short*)d_out);
}
```

```cpp
#include <hip/hip_runtime.h>
#include <stdint.h>

typedef __bf16 bf16x8 __attribute__((ext_vector_type(8)));
typedef __bf16 bf16x2 __attribute__((ext_vector_type(2)));
typedef float f32x2 __attribute__((ext_vector_type(2)));
typedef float f32x4 __attribute__((ext_vector_type(4)));
typedef float f32x16 __attribute__((ext_vector_type(16)));
typedef unsigned u32x4 __attribute__((ext_vector_type(4)));
typedef unsigned u32x2 __attribute__((ext_vector_type(2)));
typedef short s16x4 __attribute__((ext_vector_type(4)));
#define LDS_AS __attribute__((address_space(3)))
#define GLB_AS __attribute__((address_space(1)))

constexpr size_t WS_PP   = 0;
constexpr size_t WS_PHIQ = (size_t)8 << 20;
constexpr size_t WS_KVP  = (size_t)12 << 20;
constexpr size_t WS_WD   = (size_t)16 << 20;
constexpr size_t WS_WO   = (size_t)17 << 20;
constexpr size_t WS_KVZ  = ((size_t)17 << 20) + ((size_t)512 << 10);
constexpr size_t WS_END  = (size_t)18 << 20;

constexpr int NS = 5;
constexpr int STAGE_B = 32768;
constexpr int K1_LDS = NS * STAGE_B;
#define PFD 4
constexpr int NT_A = 8, NT_B = 16, NT = NT_A + NT_B;

constexpr bool st_isA(int i) { return i % 3 == 0; }
constexpr int st_a(int i) { return i / 3; }
constexpr int st_b(int i) { return (i / 3) * 2 + (i % 3) - 1; }
constexpr int LAST_A = 21;

__device__ __forceinline__ unsigned pk2(float a, float b) {
    f32x2 v = {a, b};
    return __builtin_bit_cast(unsigned, __builtin_convertvector(v, bf16x2));
}
__device__ __forceinline__ float bflo(unsigned u) { return __builtin_bit_cast(float, u << 16); }
__device__ __forceinline__ float bfhi(unsigned u) { return __builtin_bit_cast(float, u & 0xffff0000u); }
__device__ __forceinline__ f32x16 mfma32(u32x4 a, u32x4 b, f32x16 c) {
    return __builtin_amdgcn_mfma_f32_32x32x16_bf16(__builtin_bit_cast(bf16x8, a), __builtin_bit_cast(bf16x8, b), c, 0, 0, 0);
}
__device__ __forceinline__ u32x4 lds_r128(unsigned addr) {
    return *(const LDS_AS u32x4*)(uintptr_t)addr; }
__device__ __forceinline__ u32x2 lds_tr64(unsigned addr) {
    s16x4 v = __builtin_amdgcn_ds_read_tr16_b64_v4i16((LDS_AS s16x4*)(uintptr_t)addr);
    return __builtin_bit_cast(u32x2, v);
}
__device__ __forceinline__ u32x4 lds_trfrag(unsigned addr) {
    u32x2 lo = lds_tr64(addr), hi = lds_tr64(addr + 512);
    u32x4 r = {lo[0], lo[1], hi[0], hi[1]};
    return r;
}
__device__ __forceinline__ void glds16(const char* g, unsigned lds) {
    __builtin_amdgcn_global_load_lds((const GLB_AS void*)g, (LDS_AS void*)(uintptr_t)lds, 16, 0, 0);
}
#define STAMP(k, i) do {} while (0)
#define CSTAMP(k, i) do {} while (0)
#define BARRIER() asm volatile("s_barrier" ::: "memory")
#define WAIT_LGKM0_BARRIER() asm volatile("s_waitcnt lgkmcnt(0)\n\ts_barrier" ::: "memory")
#define WAIT_VM(n) asm volatile("s_waitcnt vmcnt(" #n ")" ::: "memory")

#define SB() __builtin_amdgcn_sched_barrier(0)
#define ST_AUX 16
__device__ __forceinline__ u32x4 gld(const void* p) {
    return *(const u32x4*)p;
}
__device__ __forceinline__ void lds_w128(unsigned addr, u32x4 v) {
    *(LDS_AS u32x4*)(uintptr_t)addr = v;
}

__device__ __forceinline__ void softmax16(const f32x16& p, u32x4& o0, u32x4& o1) {
    float x[16];
#pragma unroll
    for (int i = 0; i < 8; ++i) { unsigned u = pk2(p[2 * i], p[2 * i + 1]); x[2 * i] = bflo(u); x[2 * i + 1] = bfhi(u); }
    float m = fmaxf(x[0], x[1]);
#pragma unroll
    for (int i = 1; i < 8; ++i) m = fmaxf(fmaxf(m, x[2 * i]), x[2 * i + 1]);
    const float L2E = 1.4426950408889634f;
    const float mb = -m * L2E;
    float s0 = 0.f, s1 = 0.f;
#pragma unroll
    for (int i = 0; i < 8; ++i) {
        x[2 * i] = __builtin_amdgcn_exp2f(__builtin_fmaf(x[2 * i], L2E, mb)); s0 += x[2 * i];
        x[2 * i + 1] = __builtin_amdgcn_exp2f(__builtin_fmaf(x[2 * i + 1], L2E, mb)); s1 += x[2 * i + 1];
    }
    const float s = s0 + s1;
    float inv = __builtin_amdgcn_rcpf(s);
#pragma unroll
    for (int i = 0; i < 4; ++i) { o0[i] = pk2(x[2 * i] * inv, x[2 * i + 1] * inv); o1[i] = pk2(x[8 + 2 * i] * inv, x[9 + 2 * i] * inv); }
}
__device__ __forceinline__ u32x4 pack8(const f32x16& a, int o) {
    u32x4 r;
#pragma unroll
    for (int i = 0; i < 4; ++i) r[i] = pk2(a[o + 2 * i], a[o + 2 * i + 1]);
    return r;
}

__global__ __launch_bounds__(512, 2) void k1_kernel(
    const char* __restrict__ xA, const char* __restrict__ wqkv, const unsigned short* __restrict__ wphi,
    const unsigned short* __restrict__ wo, const char* __restrict__ xB, const char* __restrict__ wsre,
    const char* __restrict__ wsim, const char* __restrict__ wdre, const char* __restrict__ wdim, char* __restrict__ ws)
{
    extern __shared__ __attribute__((aligned(16))) char shm[];
    (void)shm;
    const int tid = threadIdx.x, l = tid & 63;
    const int w = __builtin_amdgcn_readfirstlane(tid >> 6);
    const int b = blockIdx.x, xcd = b & 7, idx = b >> 3;
#define MAP 0
    const int tsel = idx >> 3;
    const int tm = (tsel >> 1) * 16 + 2 * xcd + (tsel & 1);
    const int tn = (idx >> 1) & 3, kh = idx & 1;
    const int rb = 4 * xcd + (idx >> 3), hq = idx & 7;
    const int SH_R = tn, SH_T = tsel, SH_AR = hq, SH_AT = rb & 3;
    constexpr int A_RL = 8, A_TL = 48;
    const int rotA = rb & 7, rotB = (tm + (tm >> 4) * 5 + kh * 8) & 15;
    const int h = l >> 5, l31 = l & 31;
    const int g4 = l >> 4, q4 = (l >> 2) & 3, p4 = l & 3;
    constexpr unsigned SCR = ((LAST_A - 1) % NS) * STAGE_B;

    if (w >= 4) {
        const int lw = w - 4;
        STAMP(0, 0);
        const unsigned r8 = l >> 3, swT = (unsigned)(((l & 7) ^ (((l >> 4) & 1) << 2)) << 4);
        const unsigned swR0 = (unsigned)(((l & 7) ^ (l >> 4)) << 4), swR1 = (unsigned)(((l & 7) ^ ((l >> 4) + 4)) << 4);
        const char* aR = xA + (size_t)(64 * rb) * 1024;
        const char* aT = wqkv + (size_t)(64 * hq) * 2;
        const unsigned aRv0 = (16 * lw + r8) * 1024 + swR0, aRv1 = (16 * lw + 8 + r8) * 1024 + swR1;
        const unsigned aTv0 = (16 * lw + r8) * 3072 + swT, aTv1 = (16 * lw + 8 + r8) * 3072 + swT;
        const char* bR = (tm < 16 ? wsre : wsim) + (size_t)(128 * (tm & 15)) * 4096 + (size_t)kh * 2048;
        const char* bT = xB + (size_t)(1024 * kh) * 1024 + (size_t)(128 * tn) * 2;
        const unsigned bRv0 = (32 * lw + r8) * 4096 + swR0, bRv1 = (32 * lw + 8 + r8) * 4096 + swR1;
        const unsigned bRv2 = bRv0 + 16 * 4096, bRv3 = bRv1 + 16 * 4096;
        const unsigned bTv0 = (16 * lw + r8) * 1024 + swT, bTv1 = (16 * lw + 8 + r8) * 1024 + swT;
        auto issue = [&](int st) {
            const unsigned buf = (unsigned)(st % NS) * STAGE_B;
            if (st_isA(st)) {
                const int sa = (st_a(st) + rotA) & 7;
                const char* R = aR + sa * 128;
                const char* T = aT + (size_t)sa * (64 * 3072);
                glds16(R + aRv0, buf + (2 * lw) * 1024);
                glds16(R + aRv1, buf + (2 * lw + 1) * 1024);
#pragma unroll
                for (int u = 0; u < 3; ++u) {
                    glds16(T + u * 1024 + aTv0, buf + 8192 + u * 8192 + (2 * lw) * 1024);
                    glds16(T + u * 1024 + aTv1, buf + 8192 + u * 8192 + (2 * lw + 1) * 1024);
                }
            } else {
                const int s2 = (st_b(st) + rotB) & 15;
                const char* R = bR + s2 * 128;
                const char* T = bT + (size_t)s2 * (64 * 1024);
                glds16(R + bRv0, buf + (4 * lw) * 1024);
                glds16(R + bRv1, buf + (4 * lw + 1) * 1024);
                glds16(R + bRv2, buf + (4 * lw + 2) * 1024);
                glds16(R + bRv3, buf + (4 * lw + 3) * 1024);
#pragma unroll
                for (int u = 0; u < 2; ++u) {
                    glds16(T + u * 128 + bTv0, buf + 16384 + u * 8192 + (2 * lw) * 1024);
                    glds16(T + u * 128 + bTv1, buf + 16384 + u * 8192 + (2 * lw + 1) * 1024);
                }
            }
        };
        issue(0); issue(1);
        WAIT_VM(8);
        BARRIER();
        issue(2); issue(3);
        STAMP(0, 1);
#pragma unroll
        for (int i = 0; i <= LAST_A; ++i) {
            if (i <= NT - 4) { WAIT_VM(16); } else { WAIT_VM(8); }
            BARRIER();
            if (i + 4 < NT) issue(i + 4);
        }
        BARRIER();
        STAMP(0, 3);
        const unsigned scr = SCR + (unsigned)lw * 8192;
        f32x16 kvd[2];
        {
            const u32x4 vf0 = lds_r128(scr + 2048 + l * 16), vf1 = lds_r128(scr + 3072 + l * 16);
            const u32x4 ones = {0x3F803F80u, 0x3F803F80u, 0x3F803F80u, 0x3F803F80u};
            const f32x16 zero = {};
#pragma unroll
            for (int hh = 0; hh < 2; ++hh) {
                const u32x4 kf = lds_r128(scr + hh * 1024 + l * 16), wf = lds_r128(scr + 4096 + hh * 1024 + l * 16);
                u32x4 o0, o1;
                f32x16 pk = mfma32(wf, kf, zero);
                softmax16(pk, o0, o1);
                const unsigned wa = scr + 6144 + l31 * 64 + h * 32;
                *(LDS_AS u32x4*)(uintptr_t)wa = o0; *(LDS_AS u32x4*)(uintptr_t)(wa + 16) = o1;
                asm volatile("s_waitcnt lgkmcnt(0)" ::: "memory");
                const bool mine = (l31 >> 4) == hh;
                const u32x4 b0 = mine ? vf0 : ones, b1 = mine ? vf1 : ones;
                const unsigned ra = scr + 6144 + (unsigned)(4 * (g4 >> 1) + q4) * 64 + (unsigned)(g4 & 1) * 32 + p4 * 8;
                u32x2 a0 = lds_tr64(ra), a1 = lds_tr64(ra + 512), a2 = lds_tr64(ra + 1024), a3 = lds_tr64(ra + 1536);
                u32x4 f0 = {a0[0], a0[1], a1[0], a1[1]}, f1 = {a2[0], a2[1], a3[0], a3[1]};
                f32x16 d = mfma32(f0, b0, zero);
                d = mfma32(f1, b1, d);
                kvd[hh] = d;
                asm volatile("s_waitcnt lgkmcnt(0)" ::: "memory");
                if (hh == 0) { WAIT_VM(0); BARRIER(); }
            }
        }
        if (lw & 1) {
#pragma unroll
            for (int hh = 0; hh < 2; ++hh)
#pragma unroll
                for (int q = 0; q < 4; ++q) {
                    f32x4 v = {kvd[hh][4 * q], kvd[hh][4 * q + 1], kvd[hh][4 * q + 2], kvd[hh][4 * q + 3]};
                    *(LDS_AS f32x4*)(uintptr_t)(scr + (hh * 4 + q) * 1024 + l * 16) = v;
                }
        }
        STAMP(0, 4);
        WAIT_LGKM0_BARRIER();
        if (!(lw & 1)) {
#pragma unroll
            for (int hh = 0; hh < 2; ++hh) {
                const int head = 4 * hq + (lw & 2) + hh;
                const int nl = (l31 - 16 * hh) & 31;
                char* dst = ws + WS_KVP + ((size_t)(rb * 32 + head) * 4) * 1024 + (32 * h + nl) * 16;
#pragma unroll
                for (int q = 0; q < 4; ++q) {
                    f32x4 v = *(const LDS_AS f32x4*)(uintptr_t)(scr + 8192 + (hh * 4 + q) * 1024 + l * 16);
                    v[0] += kvd[hh][4 * q]; v[1] += kvd[hh][4 * q + 1]; v[2] += kvd[hh][4 * q + 2]; v[3] += kvd[hh][4 * q + 3];
                    if (nl <= 16) *(f32x4*)(dst + q * 1024) = v;
                }
            }
        }
        STAMP(0, 2);
        return;
    }

    STAMP(0, 0);
    const int sbk = w & 1, hp = w >> 1;
    const int fwd = b * 4 + w;
    const int gwo = b * 2 + (w & 1);
    u32x2 wda, wdc;
    unsigned short wov[8], wpv[2][8];
    {
        const int ri = fwd >> 9, eb = (fwd >> 5) & 15, db = fwd & 31;
        const char* src = (ri ? wdim : wdre) + (size_t)(32 * eb + l31) * 1024 + (16 * db + 4 * h) * 2;
        wda = *(const u32x2*)src; wdc = *(const u32x2*)(src + 16);
        const unsigned short* s2 = wo + (size_t)(16 * (gwo & 31) + 4 * h) * 512 + 32 * (gwo >> 5) + l31;
#pragma unroll
        for (int j = 0; j < 8; ++j) wov[j] = s2[((j & 3) + 8 * (j >> 2)) * 512];
        const int fr = (l31 & 3) + 4 * (l31 >> 3);
#pragma unroll
        for (int hh = 0; hh < 2; ++hh) {
            const unsigned short* s = wphi + (4 * hq + 2 * hp + hh) * 256 + (4 * h) * 16 + fr;
#pragma unroll
            for (int j = 0; j < 8; ++j) wpv[hh][j] = s[((j & 3) + 8 * (j >> 2)) * 16];
        }
        SB();
    }
    u32x4 wf[2];
    {
        const unsigned sg = (fwd >> 9) ? 0x80008000u : 0u;
        u32x4 v = {wda[0] ^ sg, wda[1] ^ sg, wdc[0] ^ sg, wdc[1] ^ sg};
        *(u32x4*)(ws + WS_WD + (size_t)fwd * 1024 + l * 16) = v;
        if (w < 2) {
            u32x4 o;
#pragma unroll
            for (int j = 0; j < 4; ++j) o[j] = (unsigned)wov[2 * j] | ((unsigned)wov[2 * j + 1] << 16);
            *(u32x4*)(ws + WS_WO + (size_t)gwo * 1024 + l * 16) = o;
        }
        if (w == 2 && l < 16) {
            const u32x4 poison = {0xFFFFFFFFu, 0xFFFFFFFFu, 0xFFFFFFFFu, 0xFFFFFFFFu};
            *(u32x4*)(ws + WS_KVZ + b * 256 + l * 16) = poison;
        }
        const unsigned sp = ((l31 >> 2) & 1) ? 0x80008000u : 0u;
#pragma unroll
        for (int hh = 0; hh < 2; ++hh)
#pragma unroll
            for (int j = 0; j < 4; ++j) wf[hh][j] = ((unsigned)wpv[hh][2 * j] | ((unsigned)wpv[hh][2 * j + 1] << 16)) ^ sp;
        SB();
    }
    const unsigned swz = (unsigned)((l31 >> 1) & 7);
    const unsigned rlane = (unsigned)l31 * 128 + ((swz ^ (unsigned)h) << 4);
    const unsigned tl = (unsigned)(8 * (g4 >> 1) + q4) * 128 + (unsigned)(2 * (g4 & 1) + (p4 >> 1)) * 16 + 8 * (p4 & 1);
    const unsigned tlane0 = tl + ((unsigned)(q4 >> 1) << 6);
    const unsigned tlane1 = tl + ((unsigned)(1 ^ (q4 >> 1)) << 6);

    f32x16 aq = {}, ak = {}, av = {}, c00 = {}, c01 = {}, c10 = {}, c11 = {};
    const int wr = w & 1, wc = w >> 1;
    unsigned rA = (unsigned)sbk * 4096 + rlane, tA = 8192 + (hp ? tlane1 : tlane0);
    unsigned rB = (unsigned)wr * 8192 + rlane;
    unsigned tB0 = 16384 + (unsigned)wc * 8192 + tlane0, tB1 = 16384 + (unsigned)wc * 8192 + tlane1;
#define OPQ() asm volatile("" : "+v"(rA), "+v"(tA), "+v"(rB), "+v"(tB0), "+v"(tB1))
    u32x4 F00, F01, F02, F03, F10, F11, F12, F13, F20, F21, F22, F23, F30, F31, F32, F33;
#define SLOT(st) (((st) % NS) * 32768)
#define RD01(st, k) do { \
        if (st_isA(st)) { F##k##0 = lds_r128((rA ^ (k << 5)) + SLOT(st)); F##k##1 = lds_trfrag(tA + SLOT(st) + k * 2048); } \
        else            { F##k##0 = lds_r128((rB ^ (k << 5)) + SLOT(st)); F##k##2 = lds_trfrag(tB0 + SLOT(st) + k * 2048); } } while (0)
#define RD23(st, k) do { \
        if (st_isA(st)) { F##k##2 = lds_trfrag(tA + SLOT(st) + k * 2048 + 8192); F##k##3 = lds_trfrag(tA + SLOT(st) + k * 2048 + 16384); } \
        else            { F##k##1 = lds_r128((rB ^ (k << 5)) + SLOT(st) + 4096); F##k##3 = lds_trfrag(tB1 + SLOT(st) + k * 2048); } } while (0)
#define STEP(st, k, nst, nk) do { \
        if (st_isA(st)) { \
            aq = mfma32(F##k##1, F##k##0, aq); SB(); \
            RD01(nst, nk); SB(); \
            ak = mfma32(F##k##2, F##k##0, ak); SB(); \
            RD23(nst, nk); SB(); \
            av = mfma32(F##k##0, F##k##3, av); SB(); \
        } else { \
            c00 = mfma32(F##k##2, F##k##0, c00); SB(); \
            RD01(nst, nk); SB(); \
            c01 = mfma32(F##k##2, F##k##1, c01); SB(); \
            RD23(nst, nk); SB(); \
            c10 = mfma32(F##k##3, F##k##0, c10); SB(); \
            c11 = mfma32(F##k##3, F##k##1, c11); SB(); \
        } } while (0)
#define STAGE(i) do { OPQ(); STEP(i, 0, i, 2); STEP(i, 1, i, 3); BARRIER(); SB(); STEP(i, 2, (i) + 1, 0); STEP(i, 3, (i) + 1, 1); } while (0)
#define STAGE3(i) do { STAGE(i); STAGE((i) + 1); STAGE((i) + 2); } while (0)
    STAMP(0, 1);
    BARRIER();
    STAMP(0, 2);
    RD01(0, 0); RD23(0, 0); RD01(0, 1); RD23(0, 1);
    SB();
    STAGE3(0); STAGE3(3); STAGE3(6); STAGE3(9); STAGE3(12); STAGE3(15); STAGE3(18);
    STAGE(21);
    {
        STAMP(0, 3);
        const unsigned scr = SCR + (unsigned)w * 8192 + l * 16;
        *(LDS_AS u32x4*)(uintptr_t)(scr) = pack8(ak, 0);
        *(LDS_AS u32x4*)(uintptr_t)(scr + 1024) = pack8(ak, 8);
        *(LDS_AS u32x4*)(uintptr_t)(scr + 2048) = pack8(av, 0);
        *(LDS_AS u32x4*)(uintptr_t)(scr + 3072) = pack8(av, 8);
        *(LDS_AS u32x4*)(uintptr_t)(scr + 4096) = wf[0];
        *(LDS_AS u32x4*)(uintptr_t)(scr + 5120) = wf[1];
        asm volatile("s_waitcnt lgkmcnt(0)\n\ts_barrier" ::: "memory");
        SB();
    }
    STAGE(22); STAGE(23);
    STAMP(0, 5);
    {
        const auto rs = __builtin_amdgcn_make_buffer_rsrc((void*)(ws + WS_PP), 0, 8 << 20, 0x00020000);
        const unsigned o = (unsigned)((kh * 128 + 4 * tm + 2 * wr) * 32 + 8 * tn + 4 * wc) * 1024 + l * 16;
        __builtin_amdgcn_raw_buffer_store_b128(pack8(c00, 0), rs, o, 0, ST_AUX);
        __builtin_amdgcn_raw_buffer_store_b128(pack8(c00, 8), rs, o + 1024, 0, ST_AUX);
        __builtin_amdgcn_raw_buffer_store_b128(pack8(c10, 0), rs, o + 2048, 0, ST_AUX);
        __builtin_amdgcn_raw_buffer_store_b128(pack8(c10, 8), rs, o + 3072, 0, ST_AUX);
        __builtin_amdgcn_raw_buffer_store_b128(pack8(c01, 0), rs, o + 32768, 0, ST_AUX);
        __builtin_amdgcn_raw_buffer_store_b128(pack8(c01, 8), rs, o + 32768 + 1024, 0, ST_AUX);
        __builtin_amdgcn_raw_buffer_store_b128(pack8(c11, 0), rs, o + 32768 + 2048, 0, ST_AUX);
        __builtin_amdgcn_raw_buffer_store_b128(pack8(c11, 8), rs, o + 32768 + 3072, 0, ST_AUX);
    }
    STAMP(0, 4);
    {
        const int sb = 2 * rb + sbk;
#pragma unroll
        for (int hh = 0; hh < 2; ++hh) {
            const int head = 4 * hq + 2 * hp + hh;
            const f32x16 zero = {};
            u32x4 o0, o1;
            f32x16 pq = mfma32(wf[hh], pack8(aq, 8 * hh), zero);
            softmax16(pq, o0, o1);
            const auto rs = __builtin_amdgcn_make_buffer_rsrc((void*)(ws + WS_PHIQ), 0, 4 << 20, 0x00020000);
            const unsigned o = (unsigned)((sb * 32 + head) * 2) * 1024 + l * 16;
            __builtin_amdgcn_raw_buffer_store_b128(o0, rs, o, 0, ST_AUX);
            __builtin_amdgcn_raw_buffer_store_b128(o1, rs, o + 1024, 0, ST_AUX);
        }
    }
    STAMP(0, 6);
}

constexpr int K2_LDS = NS * STAGE_B;

__device__ __forceinline__ void store_tiles64(const f32x16& c0, const f32x16& c1, unsigned patch, unsigned short* gp  , int l) {
    const int h = l >> 5, l31 = l & 31;
    const unsigned wa = patch + l31 * 144 + h * 8;
#pragma unroll
    for (int q = 0; q < 4; ++q) {
        u32x2 a = {pk2(c0[4 * q], c0[4 * q + 1]), pk2(c0[4 * q + 2], c0[4 * q + 3])};
        u32x2 b = {pk2(c1[4 * q], c1[4 * q + 1]), pk2(c1[4 * q + 2], c1[4 * q + 3])};
        *(LDS_AS u32x2*)(uintptr_t)(wa + q * 16) = a;
        *(LDS_AS u32x2*)(uintptr_t)(wa + 64 + q * 16) = b;
    }
    asm volatile("s_waitcnt lgkmcnt(0)" ::: "memory");
#pragma unroll
    for (int p = 0; p < 4; ++p) {
        const int row = 8 * p + (l >> 3), ch = l & 7;
        const u32x4 v = *(const LDS_AS u32x4*)(uintptr_t)(patch + row * 144 + ch * 16);
        *(u32x4*)(gp + (size_t)row * 1024 + ch * 8) = v;
    }
}

__global__ __launch_bounds__(512, 2) void k2_kernel(char* __restrict__ ws, unsigned short* __restrict__ out)
{
    extern __shared__ __attribute__((aligned(16))) char shm[];
    (void)shm;
    const int tid = threadIdx.x, l = tid & 63;
    const int w = __builtin_amdgcn_readfirstlane(tid >> 6);
    const int b = blockIdx.x, xcd = b & 7, idx = b >> 3;
    const int role = idx & 1, j16 = idx >> 1;
    const int tm = 4 * xcd + (j16 >> 2), tn = j16 & 3;
    const int h = l >> 5, l31 = l & 31;
    STAMP(1, 0);
    if (role == 0) {
        if (w >= 4) {
            const int lw = w - 4;
            const char* s0; const char* s1; size_t rstep;
            if (lw < 2) {
                s0 = ws + WS_PP + ((size_t)((lw * 128 + 2 * tm) * 32)) * 1024 + l * 16; s1 = s0 + 32768; rstep = (size_t)64 * 32768;
            } else {
                s0 = ws + WS_WD + ((size_t)((4 * tn + 2 * (lw - 2)) * 32)) * 1024 + l * 16; s1 = s0 + 32768; rstep = (size_t)16 * 32768;
            }
            auto issue = [&](int st) {
                const unsigned buf = (unsigned)(st % NS) * STAGE_B + (unsigned)lw * 8192;
                const size_t o = (size_t)(st >> 3) * rstep + (size_t)(st & 7) * 4096;
#pragma unroll
                for (int k = 0; k < 4; ++k) glds16(s0 + o + k * 1024, buf + k * 1024);
#pragma unroll
                for (int k = 0; k < 4; ++k) glds16(s1 + o + k * 1024, buf + 4096 + k * 1024);
            };
            issue(0); issue(1);
            WAIT_VM(8);
            BARRIER();
            issue(2); issue(3);
#pragma unroll
            for (int i = 0; i < 16; ++i) {
                if (i <= 12) { WAIT_VM(16); } else if (i == 13) { WAIT_VM(8); } else { WAIT_VM(0); }
                BARRIER();
                if (i + 4 < 16) issue(i + 4);
            }
            STAMP(1, 3);
            return;
        }
        const int tb = w & 1, eh = w >> 1;
        const unsigned pP0 = (unsigned)tb * 4096 + l * 16, pP1 = 8192 + (unsigned)tb * 4096 + l * 16;
        const unsigned pW0 = 16384 + (unsigned)eh * 8192 + l * 16, pW1 = pW0 + 4096;
        f32x16 c0 = {}, c1 = {};
        u32x4 p0a, p1a, w0a, w1a, p0b, p1b, w0b, w1b, p0c, p1c, w0c, w1c, p0d, p1d, w0d, w1d;
#define Y_STEP(P0, P1, W0, W1, nP0, nP1, nW0, nW1, o) do { \
            c0 = mfma32(W0, P0, c0); SB(); \
            nP0 = lds_r128((o) + pP0); nW0 = lds_r128((o) + pW0); SB(); \
            c1 = mfma32(W1, P0, c1); SB(); \
            nP1 = lds_r128((o) + pP1); nW1 = lds_r128((o) + pW1); SB(); \
            c0 = mfma32(W0, P1, c0); SB(); \
            c1 = mfma32(W1, P1, c1); SB(); } while (0)
        BARRIER();
        STAMP(1, 1);
        p0a = lds_r128(pP0); p1a = lds_r128(pP1); w0a = lds_r128(pW0); w1a = lds_r128(pW1);
        p0b = lds_r128(1024 + pP0); p1b = lds_r128(1024 + pP1); w0b = lds_r128(1024 + pW0); w1b = lds_r128(1024 + pW1);
        SB();
        unsigned yb[3][4];
#pragma unroll
        for (int wd = 0; wd < 3; ++wd) { yb[wd][0] = pP0 + wd * 65536; yb[wd][1] = pP1 + wd * 65536; yb[wd][2] = pW0 + wd * 65536; yb[wd][3] = pW1 + wd * 65536; }
#undef Y_STEP
#define Y_A(st, k, j) (yb[((st) % NS) >> 1][j] + (((st) % NS) & 1) * 32768 + (k) * 1024)
#define Y_STEP(P0, P1, W0, W1, nP0, nP1, nW0, nW1, st, k) do { \
            c0 = mfma32(W0, P0, c0); SB(); \
            nP0 = lds_r128(Y_A(st, k, 0)); nW0 = lds_r128(Y_A(st, k, 2)); SB(); \
            c1 = mfma32(W1, P0, c1); SB(); \
            nP1 = lds_r128(Y_A(st, k, 1)); nW1 = lds_r128(Y_A(st, k, 3)); SB(); \
            c0 = mfma32(W0, P1, c0); SB(); \
            c1 = mfma32(W1, P1, c1); SB(); } while (0)
#pragma unroll
        for (int i = 0; i < 16; ++i) {
            Y_STEP(p0a, p1a, w0a, w1a, p0c, p1c, w0c, w1c, i, 2);
            Y_STEP(p0b, p1b, w0b, w1b, p0d, p1d, w0d, w1d, i, 3);
            BARRIER(); SB();
            Y_STEP(p0c, p1c, w0c, w1c, p0a, p1a, w0a, w1a, i + 1, 0);
            Y_STEP(p0d, p1d, w0d, w1d, p0b, p1b, w0b, w1b, i + 1, 1);
        }
        STAMP(1, 3);
        store_tiles64(c0, c1, (13 % NS) * STAGE_B + (unsigned)w * 4608, out + (size_t)(64 * tm + 32 * tb) * 1024 + 512 + 128 * tn + 64 * eh, l);
        STAMP(1, 5);
        return;
    }
    {
        if (w < 2) {
            const int sl = (xcd * 16 + j16) * 2 + w;
            const int head = sl >> 3, q = (sl >> 1) & 3, hq = sl & 1;
            const char* src = ws + WS_KVP + ((size_t)(head * 4 + q)) * 1024 + (32 * hq + (l <= 16 ? l : 16)) * 16;
            f32x4 v[32];
#pragma unroll
            for (int r = 0; r < 32; ++r) v[r] = *(const f32x4*)(src + (size_t)r * (32 * 4096));
            f32x4 s0 = {0.f, 0.f, 0.f, 0.f}, s1 = {0.f, 0.f, 0.f, 0.f};
#pragma unroll
            for (int r = 0; r < 16; ++r) { s0 += v[2 * r]; s1 += v[2 * r + 1]; }
            s0 += s1;
            const unsigned long long pv = (unsigned long long)pk2(s0[0], s0[1]) | ((unsigned long long)pk2(s0[2], s0[3]) << 32);
            if (l < 32) {
                unsigned long long* dst = (unsigned long long*)(ws + WS_KVZ + (size_t)(head * 2 + (q & 1)) * 1024 + (l + 32 * (q >> 1)) * 16 + 8 * hq);
                __hip_atomic_store(dst, pv, __ATOMIC_RELAXED, __HIP_MEMORY_SCOPE_AGENT);
            }
            SB();
        }
        BARRIER();
        STAMP(1, 1);
        u32x4 fph[4][4], fwo[16], fkv[4][2];
        {
            const char* PH = ws + WS_PHIQ + ((size_t)((2 * tm) * 32 + 4 * w) * 2) * 1024 + l * 16;
#pragma unroll
            for (int k = 0; k < 4; ++k) {
                fph[k][0] = *(const u32x4*)(PH + k * 2048); fph[k][1] = *(const u32x4*)(PH + k * 2048 + 1024);
                fph[k][2] = *(const u32x4*)(PH + 65536 + k * 2048); fph[k][3] = *(const u32x4*)(PH + 65536 + k * 2048 + 1024);
            }
            SB();
        }
        {
            const auto rs = __builtin_amdgcn_make_buffer_rsrc((void*)(ws + WS_KVZ), 0, 65536, 0x00020000);
            const unsigned kvo = (unsigned)(4 * w) * 2048 + l * 16;
            asm volatile("s_waitcnt vmcnt(0)" ::: "memory");
            for (int spin = 0; spin < (1 << 16); ++spin) {
                bool ok = true;
#pragma unroll
                for (int k = 0; k < 4; ++k) {
                    fkv[k][0] = __builtin_amdgcn_raw_buffer_load_b128(rs, kvo + k * 2048, 0, 16);
                    fkv[k][1] = __builtin_amdgcn_raw_buffer_load_b128(rs, kvo + k * 2048 + 1024, 0, 16);
                }
#pragma unroll
                for (int k = 0; k < 4; ++k)
#pragma unroll
                    for (int i = 0; i < 4; ++i) ok = ok && fkv[k][0][i] != 0xFFFFFFFFu && fkv[k][1][i] != 0xFFFFFFFFu;
                if (__all(ok)) break;
                __builtin_amdgcn_s_sleep(8);
            }
        }
        STAMP(1, 2);
        {
            const char* WO = ws + WS_WO + ((size_t)((4 * tn + (w & 3)) * 32 + 16 * (w >> 2))) * 1024 + l * 16;
#pragma unroll
            for (int k = 0; k < 16; ++k) fwo[k] = *(const u32x4*)(WO + k * 1024);
            SB();
        }
#pragma unroll
        for (int k = 0; k < 4; ++k) {
#pragma unroll
            for (int sbi = 0; sbi < 2; ++sbi) {
                f32x16 nd = {};
                nd = mfma32(fkv[k][0], fph[k][2 * sbi], nd);
                nd = mfma32(fkv[k][1], fph[k][2 * sbi + 1], nd);
                float den = bflo(pk2(nd[8], nd[8]) & 0xffffu);
                den = bflo(pk2(den + 1e-6f, 0.f) & 0xffffu);
                const float inv = __builtin_amdgcn_rcpf(den);
                u32x4 of;
#pragma unroll
                for (int jj = 0; jj < 4; ++jj) {
                    const unsigned u = pk2(nd[2 * jj], nd[2 * jj + 1]);
                    of[jj] = pk2(bflo(u) * inv, bfhi(u) * inv);
                }
                *(LDS_AS u32x4*)(uintptr_t)((unsigned)(sbi * 32 + 4 * w + k) * 1024 + l * 16) = of;
            }
        }
        WAIT_LGKM0_BARRIER();
        STAMP(1, 3);
        f32x16 c0 = {}, c1 = {};
        {
            const unsigned ob = (unsigned)(16 * (w >> 2)) * 1024 + l * 16;
#pragma unroll
            for (int k = 0; k < 16; ++k) {
                const u32x4 o0 = lds_r128(ob + k * 1024), o1 = lds_r128(ob + 32768 + k * 1024);
                c0 = mfma32(fwo[k], o0, c0);
                c1 = mfma32(fwo[k], o1, c1);
            }
        }
        STAMP(1, 4);
        {
            const unsigned xb = 65536 + (unsigned)(w >> 2) * (64 * 528) + (unsigned)l31 * 528 + (unsigned)(w & 3) * 128 + h * 16;
#pragma unroll
            for (int q = 0; q < 4; ++q) {
                *(LDS_AS f32x4*)(uintptr_t)(xb + q * 32) = (f32x4){c0[4 * q], c0[4 * q + 1], c0[4 * q + 2], c0[4 * q + 3]};
                *(LDS_AS f32x4*)(uintptr_t)(xb + 32 * 528 + q * 32) = (f32x4){c1[4 * q], c1[4 * q + 1], c1[4 * q + 2], c1[4 * q + 3]};
            }
            WAIT_LGKM0_BARRIER();
#pragma unroll
            for (int p = 0; p < 2; ++p) {
                const int row = 8 * w + 4 * p + (l >> 4), ch = l & 15;
                const unsigned ra = 65536 + (unsigned)row * 528 + ch * 32;
                f32x4 a0 = *(const LDS_AS f32x4*)(uintptr_t)(ra), a1 = *(const LDS_AS f32x4*)(uintptr_t)(ra + 16);
                const f32x4 b0 = *(const LDS_AS f32x4*)(uintptr_t)(ra + 64 * 528), b1 = *(const LDS_AS f32x4*)(uintptr_t)(ra + 64 * 528 + 16);
                a0 += b0; a1 += b1;
                const u32x4 o = {pk2(a0[0], a0[1]), pk2(a0[2], a0[3]), pk2(a1[0], a1[1]), pk2(a1[2], a1[3])};
                *(u32x4*)(out + (size_t)(64 * tm + row) * 1024 + 128 * tn + ch * 8) = o;
            }
        }
        STAMP(1, 5);
    }
}

extern "C" void kernel_launch(void* const* d_in, const int* in_sizes, int n_in,
                              void* d_out, int out_size, void* d_ws, size_t ws_size,
                              hipStream_t stream) {
    (void)in_sizes; (void)n_in; (void)out_size; (void)ws_size;
    (void)hipFuncSetAttribute((const void*)k1_kernel, hipFuncAttributeMaxDynamicSharedMemorySize, K1_LDS);
    (void)hipFuncSetAttribute((const void*)k2_kernel, hipFuncAttributeMaxDynamicSharedMemorySize, K2_LDS);
    k1_kernel<<<256, 512, K1_LDS, stream>>>(
        (const char*)d_in[0], (const char*)d_in[1], (const unsigned short*)d_in[2], (const unsigned short*)d_in[3],
        (const char*)d_in[4], (const char*)d_in[5], (const char*)d_in[6], (const char*)d_in[7], (const char*)d_in[8],
        (char*)d_ws);
    k2_kernel<<<256, 512, K2_LDS, stream>>>((char*)d_ws, (unsigned short*)d_out);
}
```

```cpp
#include <hip/hip_runtime.h>
#include <stdint.h>

typedef __bf16 bf16x8 __attribute__((ext_vector_type(8)));
typedef __bf16 bf16x2 __attribute__((ext_vector_type(2)));
typedef float f32x2 __attribute__((ext_vector_type(2)));
typedef float f32x4 __attribute__((ext_vector_type(4)));
typedef float f32x16 __attribute__((ext_vector_type(16)));
typedef unsigned u32x4 __attribute__((ext_vector_type(4)));
typedef unsigned u32x2 __attribute__((ext_vector_type(2)));
typedef short s16x4 __attribute__((ext_vector_type(4)));
#define LDS_AS __attribute__((address_space(3)))
#define GLB_AS __attribute__((address_space(1)))

constexpr size_t WS_PP   = 0;
constexpr size_t WS_PHIQ = (size_t)8 << 20;
constexpr size_t WS_KVP  = (size_t)12 << 20;
constexpr size_t WS_WD   = (size_t)16 << 20;
constexpr size_t WS_WO   = (size_t)17 << 20;
constexpr size_t WS_KVZ  = ((size_t)17 << 20) + ((size_t)512 << 10);
constexpr size_t WS_END  = (size_t)18 << 20;

constexpr int NS = 5;
constexpr int STAGE_B = 32768;
constexpr int K1_LDS = NS * STAGE_B;
#define PFD 4
constexpr int NT_A = 8, NT_B = 16, NT = NT_A + NT_B;

constexpr bool st_isA(int i) { return i % 3 == 0; }
constexpr int st_a(int i) { return i / 3; }
constexpr int st_b(int i) { return (i / 3) * 2 + (i % 3) - 1; }
constexpr int LAST_A = 21;

__device__ __forceinline__ unsigned pk2(float a, float b) {
    f32x2 v = {a, b};
    return __builtin_bit_cast(unsigned, __builtin_convertvector(v, bf16x2));
}
__device__ __forceinline__ float bflo(unsigned u) { return __builtin_bit_cast(float, u << 16); }
__device__ __forceinline__ float bfhi(unsigned u) { return __builtin_bit_cast(float, u & 0xffff0000u); }
__device__ __forceinline__ f32x16 mfma32(u32x4 a, u32x4 b, f32x16 c) {
    return __builtin_amdgcn_mfma_f32_32x32x16_bf16(__builtin_bit_cast(bf16x8, a), __builtin_bit_cast(bf16x8, b), c, 0, 0, 0);
}
__device__ __forceinline__ u32x4 lds_r128(unsigned addr) {
    return *(const LDS_AS u32x4*)(uintptr_t)addr; }
__device__ __forceinline__ u32x2 lds_tr64(unsigned addr) {
    s16x4 v = __builtin_amdgcn_ds_read_tr16_b64_v4i16((LDS_AS s16x4*)(uintptr_t)addr);
    return __builtin_bit_cast(u32x2, v);
}
__device__ __forceinline__ u32x4 lds_trfrag(unsigned addr) {
    u32x2 lo = lds_tr64(addr), hi = lds_tr64(addr + 512);
    u32x4 r = {lo[0], lo[1], hi[0], hi[1]};
    return r;
}
__device__ __forceinline__ void glds16(const char* g, unsigned lds) {
    __builtin_amdgcn_global_load_lds((const GLB_AS void*)g, (LDS_AS void*)(uintptr_t)lds, 16, 0, 0);
}
#define STAMP(k, i) do {} while (0)
#define CSTAMP(k, i) do {} while (0)
#define BARRIER() asm volatile("s_barrier" ::: "memory")
#define WAIT_LGKM0_BARRIER() asm volatile("s_waitcnt lgkmcnt(0)\n\ts_barrier" ::: "memory")
#define WS_AUX 2
__device__ __forceinline__ void glds16s(const char* g, unsigned lds) {
    __builtin_amdgcn_global_load_lds((const GLB_AS void*)g, (LDS_AS void*)(uintptr_t)lds, 16, 0, WS_AUX);
}
#define WAIT_VM(n) asm volatile("s_waitcnt vmcnt(" #n ")" ::: "memory")

#define SB() __builtin_amdgcn_sched_barrier(0)
__device__ __forceinline__ void glds16_asm(const char* sbase, unsigned voff, unsigned lds) {
    unsigned keep;
    asm volatile("s_mov_b32 %0, m0\n\ts_mov_b32 m0, %3\n\ts_nop 0\n\tglobal_load_lds_dwordx4 %1, %2\n\ts_mov_b32 m0, %0"
                 : "=&s"(keep) : "v"(voff), "s"(sbase), "s"(lds) : "memory");
}
#define ST_AUX 16
__device__ __forceinline__ u32x4 gld(const void* p) {
    return *(const u32x4*)p;
}
__device__ __forceinline__ void lds_w128(unsigned addr, u32x4 v) {
    *(LDS_AS u32x4*)(uintptr_t)addr = v;
}

__device__ __forceinline__ void softmax16(const f32x16& p, u32x4& o0, u32x4& o1) {
    float x[16];
#pragma unroll
    for (int i = 0; i < 8; ++i) { unsigned u = pk2(p[2 * i], p[2 * i + 1]); x[2 * i] = bflo(u); x[2 * i + 1] = bfhi(u); }
    float m = fmaxf(x[0], x[1]);
#pragma unroll
    for (int i = 1; i < 8; ++i) m = fmaxf(fmaxf(m, x[2 * i]), x[2 * i + 1]);
    const float L2E = 1.4426950408889634f;
    const float mb = -m * L2E;
    float s0 = 0.f, s1 = 0.f;
#pragma unroll
    for (int i = 0; i < 8; ++i) {
        x[2 * i] = __builtin_amdgcn_exp2f(__builtin_fmaf(x[2 * i], L2E, mb)); s0 += x[2 * i];
        x[2 * i + 1] = __builtin_amdgcn_exp2f(__builtin_fmaf(x[2 * i + 1], L2E, mb)); s1 += x[2 * i + 1];
    }
    const float s = s0 + s1;
    float inv = __builtin_amdgcn_rcpf(s);
#pragma unroll
    for (int i = 0; i < 4; ++i) { o0[i] = pk2(x[2 * i] * inv, x[2 * i + 1] * inv); o1[i] = pk2(x[8 + 2 * i] * inv, x[9 + 2 * i] * inv); }
}
__device__ __forceinline__ u32x4 pack8(const f32x16& a, int o) {
    u32x4 r;
#pragma unroll
    for (int i = 0; i < 4; ++i) r[i] = pk2(a[o + 2 * i], a[o + 2 * i + 1]);
    return r;
}

__global__ __launch_bounds__(512, 2) void k1_kernel(
    const char* __restrict__ xA, const char* __restrict__ wqkv, const unsigned short* __restrict__ wphi,
    const unsigned short* __restrict__ wo, const char* __restrict__ xB, const char* __restrict__ wsre,
    const char* __restrict__ wsim, const char* __restrict__ wdre, const char* __restrict__ wdim, char* __restrict__ ws)
{
    extern __shared__ __attribute__((aligned(16))) char shm[];
    (void)shm;
    const int tid = threadIdx.x, l = tid & 63;
    const int w = __builtin_amdgcn_readfirstlane(tid >> 6);
    const int b = blockIdx.x, xcd = b & 7, idx = b >> 3;
#define MAP 0
    const int tsel = idx >> 3;
    const int tm = (tsel >> 1) * 16 + 2 * xcd + (tsel & 1);
    const int tn = (idx >> 1) & 3, kh = idx & 1;
    const int rb = 4 * xcd + (idx >> 3), hq = idx & 7;
    const int SH_R = tn, SH_T = tsel, SH_AR = hq, SH_AT = rb & 3;
    constexpr int A_RL = 8, A_TL = 48;
    const int rotA = rb & 7, rotB = (tm + (tm >> 4) * 5 + kh * 8) & 15;
    const int h = l >> 5, l31 = l & 31;
    const int g4 = l >> 4, q4 = (l >> 2) & 3, p4 = l & 3;
    constexpr unsigned SCR = ((LAST_A - 1) % NS) * STAGE_B;

    if (w >= 4) {
        const int lw = w - 4;
        STAMP(0, 0);
        const unsigned r8 = l >> 3, swT = (unsigned)(((l & 7) ^ (((l >> 4) & 1) << 2)) << 4);
        const unsigned swR0 = (unsigned)(((l & 7) ^ (l >> 4)) << 4), swR1 = (unsigned)(((l & 7) ^ ((l >> 4) + 4)) << 4);
        const char* aR = xA + (size_t)(64 * rb) * 1024;
        const char* aT = wqkv + (size_t)(64 * hq) * 2;
        const unsigned aRv0 = (16 * lw + r8) * 1024 + swR0, aRv1 = (16 * lw + 8 + r8) * 1024 + swR1;
        const unsigned aTv0 = (16 * lw + r8) * 3072 + swT, aTv1 = (16 * lw + 8 + r8) * 3072 + swT;
        const char* bR = (tm < 16 ? wsre : wsim) + (size_t)(128 * (tm & 15)) * 4096 + (size_t)kh * 2048;
        const char* bT = xB + (size_t)(1024 * kh) * 1024 + (size_t)(128 * tn) * 2;
        const unsigned bRv0 = (32 * lw + r8) * 4096 + swR0, bRv1 = (32 * lw + 8 + r8) * 4096 + swR1;
        const unsigned bRv2 = bRv0 + 16 * 4096, bRv3 = bRv1 + 16 * 4096;
        const unsigned bTv0 = (16 * lw + r8) * 1024 + swT, bTv1 = (16 * lw + 8 + r8) * 1024 + swT;
        auto issue = [&](int st) {
            const unsigned buf = (unsigned)(st % NS) * STAGE_B;
            if (st_isA(st)) {
                const int sa = (st_a(st) + rotA) & 7;
                const char* R = aR + sa * 128;
                const char* T = aT + (size_t)sa * (64 * 3072);
                glds16(R + aRv0, buf + (2 * lw) * 1024);
                glds16(R + aRv1, buf + (2 * lw + 1) * 1024);
#pragma unroll
                for (int u = 0; u < 3; ++u) {
                    glds16(T + u * 1024 + aTv0, buf + 8192 + u * 8192 + (2 * lw) * 1024);
                    glds16(T + u * 1024 + aTv1, buf + 8192 + u * 8192 + (2 * lw + 1) * 1024);
                }
            } else {
                const int s2 = (st_b(st) + rotB) & 15;
                const char* R = bR + s2 * 128;
                const char* T = bT + (size_t)s2 * (64 * 1024);
                glds16s(R + bRv0, buf + (4 * lw) * 1024);
                glds16s(R + bRv1, buf + (4 * lw + 1) * 1024);
                glds16s(R + bRv2, buf + (4 * lw + 2) * 1024);
                glds16s(R + bRv3, buf + (4 * lw + 3) * 1024);
#pragma unroll
                for (int u = 0; u < 2; ++u) {
                    glds16(T + u * 128 + bTv0, buf + 16384 + u * 8192 + (2 * lw) * 1024);
                    glds16(T + u * 128 + bTv1, buf + 16384 + u * 8192 + (2 * lw + 1) * 1024);
                }
            }
        };
        issue(0); issue(1);
        WAIT_VM(8);
        BARRIER();
        issue(2); issue(3);
        STAMP(0, 1);
#pragma unroll
        for (int i = 0; i <= LAST_A; ++i) {
            if (i <= NT - 4) { WAIT_VM(16); } else { WAIT_VM(8); }
            BARRIER();
            if (i + 4 < NT) issue(i + 4);
        }
        BARRIER();
        STAMP(0, 3);
        const unsigned scr = SCR + (unsigned)lw * 8192;
        f32x16 kvd[2];
        {
            const u32x4 vf0 = lds_r128(scr + 2048 + l * 16), vf1 = lds_r128(scr + 3072 + l * 16);
            const u32x4 ones = {0x3F803F80u, 0x3F803F80u, 0x3F803F80u, 0x3F803F80u};
            const f32x16 zero = {};
#pragma unroll
            for (int hh = 0; hh < 2; ++hh) {
                const u32x4 kf = lds_r128(scr + hh * 1024 + l * 16), wf = lds_r128(scr + 4096 + hh * 1024 + l * 16);
                u32x4 o0, o1;
                f32x16 pk = mfma32(wf, kf, zero);
                softmax16(pk, o0, o1);
                const unsigned wa = scr + 6144 + l31 * 64 + h * 32;
                *(LDS_AS u32x4*)(uintptr_t)wa = o0; *(LDS_AS u32x4*)(uintptr_t)(wa + 16) = o1;
                asm volatile("s_waitcnt lgkmcnt(0)" ::: "memory");
                const bool mine = (l31 >> 4) == hh;
                const u32x4 b0 = mine ? vf0 : ones, b1 = mine ? vf1 : ones;
                const unsigned ra = scr + 6144 + (unsigned)(4 * (g4 >> 1) + q4) * 64 + (unsigned)(g4 & 1) * 32 + p4 * 8;
                u32x2 a0 = lds_tr64(ra), a1 = lds_tr64(ra + 512), a2 = lds_tr64(ra + 1024), a3 = lds_tr64(ra + 1536);
                u32x4 f0 = {a0[0], a0[1], a1[0], a1[1]}, f1 = {a2[0], a2[1], a3[0], a3[1]};
                f32x16 d = mfma32(f0, b0, zero);
                d = mfma32(f1, b1, d);
                kvd[hh] = d;
                asm volatile("s_waitcnt lgkmcnt(0)" ::: "memory");
                if (hh == 0) { WAIT_VM(0); BARRIER(); }
            }
        }
        if (lw & 1) {
#pragma unroll
            for (int hh = 0; hh < 2; ++hh)
#pragma unroll
                for (int q = 0; q < 4; ++q) {
                    f32x4 v = {kvd[hh][4 * q], kvd[hh][4 * q + 1], kvd[hh][4 * q + 2], kvd[hh][4 * q + 3]};
                    *(LDS_AS f32x4*)(uintptr_t)(scr + (hh * 4 + q) * 1024 + l * 16) = v;
                }
        }
        STAMP(0, 4);
        WAIT_LGKM0_BARRIER();
        if (!(lw & 1)) {
#pragma unroll
            for (int hh = 0; hh < 2; ++hh) {
                const int head = 4 * hq + (lw & 2) + hh;
                const int nl = (l31 - 16 * hh) & 31;
                char* dst = ws + WS_KVP + ((size_t)(rb * 32 + head) * 4) * 1024 + (32 * h + nl) * 16;
#pragma unroll
                for (int q = 0; q < 4; ++q) {
                    f32x4 v = *(const LDS_AS f32x4*)(uintptr_t)(scr + 8192 + (hh * 4 + q) * 1024 + l * 16);
                    v[0] += kvd[hh][4 * q]; v[1] += kvd[hh][4 * q + 1]; v[2] += kvd[hh][4 * q + 2]; v[3] += kvd[hh][4 * q + 3];
                    if (nl <= 16) *(f32x4*)(dst + q * 1024) = v;
                }
            }
        }
        STAMP(0, 2);
        return;
    }

    STAMP(0, 0);
    const int sbk = w & 1, hp = w >> 1;
    const int fwd = b * 4 + w;
    const int gwo = b * 2 + (w & 1);
    u32x2 wda, wdc;
    unsigned short wov[8], wpv[2][8];
    {
        const int ri = fwd >> 9, eb = (fwd >> 5) & 15, db = fwd & 31;
        const char* src = (ri ? wdim : wdre) + (size_t)(32 * eb + l31) * 1024 + (16 * db + 4 * h) * 2;
        wda = *(const u32x2*)src; wdc = *(const u32x2*)(src + 16);
        const unsigned short* s2 = wo + (size_t)(16 * (gwo & 31) + 4 * h) * 512 + 32 * (gwo >> 5) + l31;
#pragma unroll
        for (int j = 0; j < 8; ++j) wov[j] = s2[((j & 3) + 8 * (j >> 2)) * 512];
        const int fr = (l31 & 3) + 4 * (l31 >> 3);
#pragma unroll
        for (int hh = 0; hh < 2; ++hh) {
            const unsigned short* s = wphi + (4 * hq + 2 * hp + hh) * 256 + (4 * h) * 16 + fr;
#pragma unroll
            for (int j = 0; j < 8; ++j) wpv[hh][j] = s[((j & 3) + 8 * (j >> 2)) * 16];
        }
        SB();
    }
    u32x4 wf[2];
    {
        const unsigned sg = (fwd >> 9) ? 0x80008000u : 0u;
        u32x4 v = {wda[0] ^ sg, wda[1] ^ sg, wdc[0] ^ sg, wdc[1] ^ sg};
        *(u32x4*)(ws + WS_WD + (size_t)fwd * 1024 + l * 16) = v;
        if (w < 2) {
            u32x4 o;
#pragma unroll
            for (int j = 0; j < 4; ++j) o[j] = (unsigned)wov[2 * j] | ((unsigned)wov[2 * j + 1] << 16);
            *(u32x4*)(ws + WS_WO + (size_t)gwo * 1024 + l * 16) = o;
        }
        if (w == 2 && l < 16) {
            const u32x4 poison = {0xFFFFFFFFu, 0xFFFFFFFFu, 0xFFFFFFFFu, 0xFFFFFFFFu};
            *(u32x4*)(ws + WS_KVZ + b * 256 + l * 16) = poison;
        }
        const unsigned sp = ((l31 >> 2) & 1) ? 0x80008000u : 0u;
#pragma unroll
        for (int hh = 0; hh < 2; ++hh)
#pragma unroll
            for (int j = 0; j < 4; ++j) wf[hh][j] = ((unsigned)wpv[hh][2 * j] | ((unsigned)wpv[hh][2 * j + 1] << 16)) ^ sp;
        SB();
    }
    const unsigned swz = (unsigned)((l31 >> 1) & 7);
    const unsigned rlane = (unsigned)l31 * 128 + ((swz ^ (unsigned)h) << 4);
    const unsigned tl = (unsigned)(8 * (g4 >> 1) + q4) * 128 + (unsigned)(2 * (g4 & 1) + (p4 >> 1)) * 16 + 8 * (p4 & 1);
    const unsigned tlane0 = tl + ((unsigned)(q4 >> 1) << 6);
    const unsigned tlane1 = tl + ((unsigned)(1 ^ (q4 >> 1)) << 6);

    f32x16 aq = {}, ak = {}, av = {}, c00 = {}, c01 = {}, c10 = {}, c11 = {};
    const int wr = w & 1, wc = w >> 1;
    unsigned rA = (unsigned)sbk * 4096 + rlane, tA = 8192 + (hp ? tlane1 : tlane0);
    unsigned rB = (unsigned)wr * 8192 + rlane;
    unsigned tB0 = 16384 + (unsigned)wc * 8192 + tlane0, tB1 = 16384 + (unsigned)wc * 8192 + tlane1;
    unsigned rX0, rX1, rX2, rX3, tX0, tX1;
#define SLOT(st) (((st) % NS) * 32768)
#define SETB(st) do { \
        asm volatile("" : "+v"(rA), "+v"(tA), "+v"(rB), "+v"(tB0), "+v"(tB1)); \
        if (st_isA(st)) { rX0 = rA + SLOT(st); tX0 = tA + SLOT(st); tX1 = tX0; } \
        else            { rX0 = rB + SLOT(st); tX0 = tB0 + SLOT(st); tX1 = tB1 + SLOT(st); } \
        rX1 = rX0 ^ 32; rX2 = rX0 ^ 64; rX3 = rX0 ^ 96; \
        asm volatile("" : "+v"(rX0), "+v"(rX1), "+v"(rX2), "+v"(rX3), "+v"(tX0), "+v"(tX1)); SB(); } while (0)
    u32x4 F00, F01, F02, F03, F10, F11, F12, F13, F20, F21, F22, F23, F30, F31, F32, F33;
#define RD01(st, k) do { \
        if (st_isA(st)) { F##k##0 = lds_r128(rX##k); F##k##1 = lds_trfrag(tX0 + k * 2048); } \
        else            { F##k##0 = lds_r128(rX##k); F##k##2 = lds_trfrag(tX0 + k * 2048); } } while (0)
#define RD23(st, k) do { \
        if (st_isA(st)) { F##k##2 = lds_trfrag(tX0 + k * 2048 + 8192); F##k##3 = lds_trfrag(tX0 + k * 2048 + 16384); } \
        else            { F##k##1 = lds_r128(rX##k + 4096); F##k##3 = lds_trfrag(tX1 + k * 2048); } } while (0)
#define STEP(st, k, nst, nk) do { \
        if (st_isA(st)) { \
            aq = mfma32(F##k##1, F##k##0, aq); SB(); \
            RD01(nst, nk); SB(); \
            ak = mfma32(F##k##2, F##k##0, ak); SB(); \
            RD23(nst, nk); SB(); \
            av = mfma32(F##k##0, F##k##3, av); SB(); \
        } else { \
            c00 = mfma32(F##k##2, F##k##0, c00); SB(); \
            RD01(nst, nk); SB(); \
            c01 = mfma32(F##k##2, F##k##1, c01); SB(); \
            RD23(nst, nk); SB(); \
            c10 = mfma32(F##k##3, F##k##0, c10); SB(); \
            c11 = mfma32(F##k##3, F##k##1, c11); SB(); \
        } } while (0)
#define STAGE(i) do { STEP(i, 0, i, 2); STEP(i, 1, i, 3); SETB((i) + 1); BARRIER(); SB(); STEP(i, 2, (i) + 1, 0); STEP(i, 3, (i) + 1, 1); } while (0)
#define STAGE3(i) do { STAGE(i); STAGE((i) + 1); STAGE((i) + 2); } while (0)
    STAMP(0, 1);
    BARRIER();
    STAMP(0, 2);
    SETB(0);
    RD01(0, 0); RD23(0, 0); RD01(0, 1); RD23(0, 1);
    SB();
    STAGE3(0); STAGE3(3); STAGE3(6); STAGE3(9); STAGE3(12); STAGE3(15); STAGE3(18);
    STAGE(21);
    {
        STAMP(0, 3);
        const unsigned scr = SCR + (unsigned)w * 8192 + l * 16;
        *(LDS_AS u32x4*)(uintptr_t)(scr) = pack8(ak, 0);
        *(LDS_AS u32x4*)(uintptr_t)(scr + 1024) = pack8(ak, 8);
        *(LDS_AS u32x4*)(uintptr_t)(scr + 2048) = pack8(av, 0);
        *(LDS_AS u32x4*)(uintptr_t)(scr + 3072) = pack8(av, 8);
        *(LDS_AS u32x4*)(uintptr_t)(scr + 4096) = wf[0];
        *(LDS_AS u32x4*)(uintptr_t)(scr + 5120) = wf[1];
        asm volatile("s_waitcnt lgkmcnt(0)\n\ts_barrier" ::: "memory");
        SB();
    }
    STAGE(22); STAGE(23);
    STAMP(0, 5);
    {
        const auto rs = __builtin_amdgcn_make_buffer_rsrc((void*)(ws + WS_PP), 0, 8 << 20, 0x00020000);
        const unsigned o = (unsigned)((kh * 128 + 4 * tm + 2 * wr) * 32 + 8 * tn + 4 * wc) * 1024 + l * 16;
        __builtin_amdgcn_raw_buffer_store_b128(pack8(c00, 0), rs, o, 0, ST_AUX);
        __builtin_amdgcn_raw_buffer_store_b128(pack8(c00, 8), rs, o + 1024, 0, ST_AUX);
        __builtin_amdgcn_raw_buffer_store_b128(pack8(c10, 0), rs, o + 2048, 0, ST_AUX);
        __builtin_amdgcn_raw_buffer_store_b128(pack8(c10, 8), rs, o + 3072, 0, ST_AUX);
        __builtin_amdgcn_raw_buffer_store_b128(pack8(c01, 0), rs, o + 32768, 0, ST_AUX);
        __builtin_amdgcn_raw_buffer_store_b128(pack8(c01, 8), rs, o + 32768 + 1024, 0, ST_AUX);
        __builtin_amdgcn_raw_buffer_store_b128(pack8(c11, 0), rs, o + 32768 + 2048, 0, ST_AUX);
        __builtin_amdgcn_raw_buffer_store_b128(pack8(c11, 8), rs, o + 32768 + 3072, 0, ST_AUX);
    }
    STAMP(0, 4);
    {
        const int sb = 2 * rb + sbk;
#pragma unroll
        for (int hh = 0; hh < 2; ++hh) {
            const int head = 4 * hq + 2 * hp + hh;
            const f32x16 zero = {};
            u32x4 o0, o1;
            f32x16 pq = mfma32(wf[hh], pack8(aq, 8 * hh), zero);
            softmax16(pq, o0, o1);
            const auto rs = __builtin_amdgcn_make_buffer_rsrc((void*)(ws + WS_PHIQ), 0, 4 << 20, 0x00020000);
            const unsigned o = (unsigned)((sb * 32 + head) * 2) * 1024 + l * 16;
            __builtin_amdgcn_raw_buffer_store_b128(o0, rs, o, 0, ST_AUX);
            __builtin_amdgcn_raw_buffer_store_b128(o1, rs, o + 1024, 0, ST_AUX);
        }
    }
    STAMP(0, 6);
}

constexpr int K2_LDS = NS * STAGE_B;

#define OUT_AUX 0
__device__ __forceinline__ void st_out(unsigned short* p, u32x4 v) {
    if (OUT_AUX == 2) __builtin_nontemporal_store(v, (u32x4*)p); else *(u32x4*)p = v;
}
__device__ __forceinline__ void store_tiles64(const f32x16& c0, const f32x16& c1, unsigned patch, unsigned short* gp  , int l) {
    const int h = l >> 5, l31 = l & 31;
    const unsigned wa = patch + l31 * 144 + h * 8;
#pragma unroll
    for (int q = 0; q < 4; ++q) {
        u32x2 a = {pk2(c0[4 * q], c0[4 * q + 1]), pk2(c0[4 * q + 2], c0[4 * q + 3])};
        u32x2 b = {pk2(c1[4 * q], c1[4 * q + 1]), pk2(c1[4 * q + 2], c1[4 * q + 3])};
        *(LDS_AS u32x2*)(uintptr_t)(wa + q * 16) = a;
        *(LDS_AS u32x2*)(uintptr_t)(wa + 64 + q * 16) = b;
    }
    asm volatile("s_waitcnt lgkmcnt(0)" ::: "memory");
#pragma unroll
    for (int p = 0; p < 4; ++p) {
        const int row = 8 * p + (l >> 3), ch = l & 7;
        const u32x4 v = *(const LDS_AS u32x4*)(uintptr_t)(patch + row * 144 + ch * 16);
        st_out(gp + (size_t)row * 1024 + ch * 8, v);
    }
}

__global__ __launch_bounds__(512, 2) void k2_kernel(char* __restrict__ ws, unsigned short* __restrict__ out)
{
    extern __shared__ __attribute__((aligned(16))) char shm[];
    (void)shm;
    const int tid = threadIdx.x, l = tid & 63;
    const int w = __builtin_amdgcn_readfirstlane(tid >> 6);
    const int b = blockIdx.x, xcd = b & 7, idx = b >> 3;
    const int role = idx & 1, j16 = idx >> 1;
    const int tm = 4 * xcd + (j16 >> 2), tn = j16 & 3;
    const int h = l >> 5, l31 = l & 31;
    STAMP(1, 0);
    if (role == 0) {
        if (w >= 4) {
            const int tbl = w & 1, kp = (w >> 1) & 1;
            const char* s0 = ws + WS_PP + ((size_t)((2 * tm + tbl) * 32 + 2 * kp)) * 1024 + l * 16;
            u32x4 g[4][4];
            auto load = [&](int st) {
                u32x4* d = g[st & 3];
                const size_t o = (size_t)(st >> 3) * ((size_t)64 * 32768) + (size_t)(st & 7) * 4096;
#pragma unroll
                for (int k = 0; k < 2; ++k) { d[2 * k] = *(const u32x4*)(s0 + o + k * 1024); d[2 * k + 1] = *(const u32x4*)(s0 + ((size_t)4 << 20) + o + k * 1024); }
                SB();
            };
            auto put = [&](int st) {
                const u32x4* d = g[st & 3];
                const unsigned buf = (unsigned)(st % NS) * STAGE_B + (unsigned)tbl * 4096 + (unsigned)kp * 2048 + l * 16;
#pragma unroll
                for (int k = 0; k < 2; ++k) {
                    u32x4 r;
#pragma unroll
                    for (int j = 0; j < 4; ++j) r[j] = pk2(bflo(d[2 * k][j]) + bflo(d[2 * k + 1][j]), bfhi(d[2 * k][j]) + bfhi(d[2 * k + 1][j]));
                    *(LDS_AS u32x4*)(uintptr_t)(buf + k * 1024) = r;
                }
                SB();
            };
            load(0); load(1); load(2); load(3);
            put(0);
            WAIT_LGKM0_BARRIER();
#pragma unroll
            for (int i = 0; i < 16; ++i) {
                if (i >= 1 && i + 3 < 16) load(i + 3);
                if (i + 1 < 16) put(i + 1);
                WAIT_LGKM0_BARRIER();
            }
            return;
        }
        const char* wdb = ws + WS_WD + ((size_t)((4 * tn + w) * 32)) * 1024;
        const unsigned wdv = (unsigned)l * 16;
        auto issue = [&](int st) {
            const unsigned buf = (unsigned)(st % NS) * STAGE_B + 8192 + (unsigned)w * 4096;
            const char* src = wdb + (size_t)(st >> 3) * ((size_t)16 * 32768) + (size_t)(st & 7) * 4096;
#pragma unroll
            for (int k = 0; k < 4; ++k) glds16_asm(src + k * 1024, wdv, buf + k * 1024);
            SB();
        };
        issue(0); issue(1); issue(2); issue(3);
        const int tb = w & 1, eh = w >> 1;
        f32x16 c0 = {}, c1 = {};
        unsigned yP = (unsigned)tb * 4096 + l * 16, yW = 8192 + (unsigned)eh * 8192 + l * 16;
        unsigned yp, yw;
        u32x4 P0, W00, W01, P1, W10, W11, P2, W20, W21, P3, W30, W31;
#define Y_SETB(st) do { asm volatile("" : "+v"(yP), "+v"(yW)); yp = yP + ((st) % NS) * 32768; yw = yW + ((st) % NS) * 32768; asm volatile("" : "+v"(yp), "+v"(yw)); SB(); } while (0)
#define Y_STEP(k, nk) do { \
            c0 = mfma32(W##k##0, P##k, c0); SB(); \
            P##nk = lds_r128(yp + nk * 1024); W##nk##0 = lds_r128(yw + nk * 1024); W##nk##1 = lds_r128(yw + 4096 + nk * 1024); SB(); \
            c1 = mfma32(W##k##1, P##k, c1); SB(); } while (0)
        WAIT_VM(12);
        BARRIER();
        STAMP(1, 1);
        Y_SETB(0);
        P0 = lds_r128(yp); W00 = lds_r128(yw); W01 = lds_r128(yw + 4096);
        P1 = lds_r128(yp + 1024); W10 = lds_r128(yw + 1024); W11 = lds_r128(yw + 4096 + 1024);
        SB();
#pragma unroll
        for (int i = 0; i < 16; ++i) {
            Y_STEP(0, 2); Y_STEP(1, 3);
            Y_SETB(i + 1);
            if (i <= 12) { WAIT_VM(8); } else if (i == 13) { WAIT_VM(4); } else { WAIT_VM(0); }
            BARRIER(); SB();
            if (i + 4 < 16) issue(i + 4);
            Y_STEP(2, 0); Y_STEP(3, 1);
        }
        STAMP(1, 3);
        store_tiles64(c0, c1, (13 % NS) * STAGE_B + (unsigned)w * 4608, out + (size_t)(64 * tm + 32 * tb) * 1024 + 512 + 128 * tn + 64 * eh, l);
        STAMP(1, 5);
        return;
    }
    {
        if (w < 2) {
            const int sl = (xcd * 16 + j16) * 2 + w;
            const int head = sl >> 3, q = (sl >> 1) & 3, hq = sl & 1;
            const char* src = ws + WS_KVP + ((size_t)(head * 4 + q)) * 1024 + (32 * hq + (l <= 16 ? l : 16)) * 16;
            f32x4 v[32];
#pragma unroll
            for (int r = 0; r < 32; ++r) v[r] = *(const f32x4*)(src + (size_t)r * (32 * 4096));
            f32x4 s0 = {0.f, 0.f, 0.f, 0.f}, s1 = {0.f, 0.f, 0.f, 0.f};
#pragma unroll
            for (int r = 0; r < 16; ++r) { s0 += v[2 * r]; s1 += v[2 * r + 1]; }
            s0 += s1;
            const unsigned long long pv = (unsigned long long)pk2(s0[0], s0[1]) | ((unsigned long long)pk2(s0[2], s0[3]) << 32);
            if (l < 32) {
                unsigned long long* dst = (unsigned long long*)(ws + WS_KVZ + (size_t)(head * 2 + (q & 1)) * 1024 + (l + 32 * (q >> 1)) * 16 + 8 * hq);
                __hip_atomic_store(dst, pv, __ATOMIC_RELAXED, __HIP_MEMORY_SCOPE_AGENT);
            }
            SB();
        }
        BARRIER();
        STAMP(1, 1);
        u32x4 fph[4][4], fwo[16], fkv[4][2];
        {
            const char* PH = ws + WS_PHIQ + ((size_t)((2 * tm) * 32 + 4 * w) * 2) * 1024 + l * 16;
#pragma unroll
            for (int k = 0; k < 4; ++k) {
                fph[k][0] = *(const u32x4*)(PH + k * 2048); fph[k][1] = *(const u32x4*)(PH + k * 2048 + 1024);
                fph[k][2] = *(const u32x4*)(PH + 65536 + k * 2048); fph[k][3] = *(const u32x4*)(PH + 65536 + k * 2048 + 1024);
            }
            SB();
        }
        {
            const auto rs = __builtin_amdgcn_make_buffer_rsrc((void*)(ws + WS_KVZ), 0, 65536, 0x00020000);
            const unsigned kvo = (unsigned)(4 * w) * 2048 + l * 16;
            asm volatile("s_waitcnt vmcnt(0)" ::: "memory");
            for (int spin = 0; spin < (1 << 16); ++spin) {
                bool ok = true;
#pragma unroll
                for (int k = 0; k < 4; ++k) {
                    fkv[k][0] = __builtin_amdgcn_raw_buffer_load_b128(rs, kvo + k * 2048, 0, 16);
                    fkv[k][1] = __builtin_amdgcn_raw_buffer_load_b128(rs, kvo + k * 2048 + 1024, 0, 16);
                }
#pragma unroll
                for (int k = 0; k < 4; ++k)
#pragma unroll
                    for (int i = 0; i < 4; ++i) ok = ok && fkv[k][0][i] != 0xFFFFFFFFu && fkv[k][1][i] != 0xFFFFFFFFu;
                if (__all(ok)) break;
                __builtin_amdgcn_s_sleep(8);
            }
        }
        STAMP(1, 2);
        {
            const char* WO = ws + WS_WO + ((size_t)((4 * tn + (w & 3)) * 32 + 16 * (w >> 2))) * 1024 + l * 16;
#pragma unroll
            for (int k = 0; k < 16; ++k) fwo[k] = *(const u32x4*)(WO + k * 1024);
            SB();
        }
#pragma unroll
        for (int k = 0; k < 4; ++k) {
#pragma unroll
            for (int sbi = 0; sbi < 2; ++sbi) {
                f32x16 nd = {};
                nd = mfma32(fkv[k][0], fph[k][2 * sbi], nd);
                nd = mfma32(fkv[k][1], fph[k][2 * sbi + 1], nd);
                float den = bflo(pk2(nd[8], nd[8]) & 0xffffu);
                den = bflo(pk2(den + 1e-6f, 0.f) & 0xffffu);
                const float inv = __builtin_amdgcn_rcpf(den);
                u32x4 of;
#pragma unroll
                for (int jj = 0; jj < 4; ++jj) {
                    const unsigned u = pk2(nd[2 * jj], nd[2 * jj + 1]);
                    of[jj] = pk2(bflo(u) * inv, bfhi(u) * inv);
                }
                *(LDS_AS u32x4*)(uintptr_t)((unsigned)(sbi * 32 + 4 * w + k) * 1024 + l * 16) = of;
            }
        }
        WAIT_LGKM0_BARRIER();
        STAMP(1, 3);
        f32x16 c0 = {}, c1 = {};
        {
            const unsigned ob = (unsigned)(16 * (w >> 2)) * 1024 + l * 16;
#pragma unroll
            for (int k = 0; k < 16; ++k) {
                const u32x4 o0 = lds_r128(ob + k * 1024), o1 = lds_r128(ob + 32768 + k * 1024);
                c0 = mfma32(fwo[k], o0, c0);
                c1 = mfma32(fwo[k], o1, c1);
            }
        }
        STAMP(1, 4);
        {
            const unsigned xb = 65536 + (unsigned)(w >> 2) * (64 * 528) + (unsigned)l31 * 528 + (unsigned)(w & 3) * 128 + h * 16;
#pragma unroll
            for (int q = 0; q < 4; ++q) {
                *(LDS_AS f32x4*)(uintptr_t)(xb + q * 32) = (f32x4){c0[4 * q], c0[4 * q + 1], c0[4 * q + 2], c0[4 * q + 3]};
                *(LDS_AS f32x4*)(uintptr_t)(xb + 32 * 528 + q * 32) = (f32x4){c1[4 * q], c1[4 * q + 1], c1[4 * q + 2], c1[4 * q + 3]};
            }
            WAIT_LGKM0_BARRIER();
#pragma unroll
            for (int p = 0; p < 2; ++p) {
                const int row = 8 * w + 4 * p + (l >> 4), ch = l & 15;
                const unsigned ra = 65536 + (unsigned)row * 528 + ch * 32;
                f32x4 a0 = *(const LDS_AS f32x4*)(uintptr_t)(ra), a1 = *(const LDS_AS f32x4*)(uintptr_t)(ra + 16);
                const f32x4 b0 = *(const LDS_AS f32x4*)(uintptr_t)(ra + 64 * 528), b1 = *(const LDS_AS f32x4*)(uintptr_t)(ra + 64 * 528 + 16);
                a0 += b0; a1 += b1;
                const u32x4 o = {pk2(a0[0], a0[1]), pk2(a0[2], a0[3]), pk2(a1[0], a1[1]), pk2(a1[2], a1[3])};
                st_out(out + (size_t)(64 * tm + row) * 1024 + 128 * tn + ch * 8, o);
            }
        }
        STAMP(1, 5);
    }
}

extern "C" void kernel_launch(void* const* d_in, const int* in_sizes, int n_in,
                              void* d_out, int out_size, void* d_ws, size_t ws_size,
                              hipStream_t stream) {
    (void)in_sizes; (void)n_in; (void)out_size; (void)ws_size;
    (void)hipFuncSetAttribute((const void*)k1_kernel, hipFuncAttributeMaxDynamicSharedMemorySize, K1_LDS);
    (void)hipFuncSetAttribute((const void*)k2_kernel, hipFuncAttributeMaxDynamicSharedMemorySize, K2_LDS);
    k1_kernel<<<256, 512, K1_LDS, stream>>>(
        (const char*)d_in[0], (const char*)d_in[1], (const unsigned short*)d_in[2], (const unsigned short*)d_in[3],
        (const char*)d_in[4], (const char*)d_in[5], (const char*)d_in[6], (const char*)d_in[7], (const char*)d_in[8],
        (char*)d_ws);
    k2_kernel<<<256, 512, K2_LDS, stream>>>((char*)d_ws, (unsigned short*)d_out);
}
```

```cpp
#include <hip/hip_runtime.h>
#include <stdint.h>

typedef __bf16 bf16x8 __attribute__((ext_vector_type(8)));
typedef __bf16 bf16x2 __attribute__((ext_vector_type(2)));
typedef float f32x2 __attribute__((ext_vector_type(2)));
typedef float f32x4 __attribute__((ext_vector_type(4)));
typedef float f32x16 __attribute__((ext_vector_type(16)));
typedef unsigned u32x4 __attribute__((ext_vector_type(4)));
typedef unsigned u32x2 __attribute__((ext_vector_type(2)));
typedef short s16x4 __attribute__((ext_vector_type(4)));
#define LDS_AS __attribute__((address_space(3)))
#define GLB_AS __attribute__((address_space(1)))

constexpr size_t WS_PP   = 0;
constexpr size_t WS_PHIQ = (size_t)8 << 20;
constexpr size_t WS_KVP  = (size_t)12 << 20;
constexpr size_t WS_WD   = (size_t)16 << 20;
constexpr size_t WS_WO   = (size_t)17 << 20;
constexpr size_t WS_KVZ  = ((size_t)17 << 20) + ((size_t)512 << 10);
constexpr size_t WS_END  = (size_t)18 << 20;

constexpr int NS = 5;
constexpr int STAGE_B = 32768;
constexpr int K1_LDS = NS * STAGE_B;
#define PFD 4
constexpr int NT_A = 8, NT_B = 16, NT = NT_A + NT_B;

constexpr bool st_isA(int i) { return i % 3 == 0; }
constexpr int st_a(int i) { return i / 3; }
constexpr int st_b(int i) { return (i / 3) * 2 + (i % 3) - 1; }
constexpr int LAST_A = 21;

__device__ __forceinline__ unsigned pk2(float a, float b) {
    f32x2 v = {a, b};
    return __builtin_bit_cast(unsigned, __builtin_convertvector(v, bf16x2));
}
__device__ __forceinline__ float bflo(unsigned u) { return __builtin_bit_cast(float, u << 16); }
__device__ __forceinline__ float bfhi(unsigned u) { return __builtin_bit_cast(float, u & 0xffff0000u); }
__device__ __forceinline__ f32x16 mfma32(u32x4 a, u32x4 b, f32x16 c) {
    return __builtin_amdgcn_mfma_f32_32x32x16_bf16(__builtin_bit_cast(bf16x8, a), __builtin_bit_cast(bf16x8, b), c, 0, 0, 0);
}
__device__ __forceinline__ u32x4 lds_r128(unsigned addr) {
    return *(const LDS_AS u32x4*)(uintptr_t)addr; }
__device__ __forceinline__ u32x2 lds_tr64(unsigned addr) {
    s16x4 v = __builtin_amdgcn_ds_read_tr16_b64_v4i16((LDS_AS s16x4*)(uintptr_t)addr);
    return __builtin_bit_cast(u32x2, v);
}
__device__ __forceinline__ u32x4 lds_trfrag(unsigned addr) {
    u32x2 lo = lds_tr64(addr), hi = lds_tr64(addr + 512);
    u32x4 r = {lo[0], lo[1], hi[0], hi[1]};
    return r;
}
__device__ __forceinline__ void glds16(const char* g, unsigned lds) {
    __builtin_amdgcn_global_load_lds((const GLB_AS void*)g, (LDS_AS void*)(uintptr_t)lds, 16, 0, 0);
}
#define STAMP(k, i) do {} while (0)
#define CSTAMP(k, i) do {} while (0)
#define BARRIER() asm volatile("s_barrier" ::: "memory")
#define WAIT_LGKM0_BARRIER() asm volatile("s_waitcnt lgkmcnt(0)\n\ts_barrier" ::: "memory")
#define WS_AUX 2
__device__ __forceinline__ void glds16s(const char* g, unsigned lds) {
    __builtin_amdgcn_global_load_lds((const GLB_AS void*)g, (LDS_AS void*)(uintptr_t)lds, 16, 0, WS_AUX);
}
#define WAIT_VM(n) asm volatile("s_waitcnt vmcnt(" #n ")" ::: "memory")

#define SB() __builtin_amdgcn_sched_barrier(0)
__device__ __forceinline__ void glds16_asm(const char* sbase, unsigned voff, unsigned lds) {
    unsigned keep;
    asm volatile("s_mov_b32 %0, m0\n\ts_mov_b32 m0, %3\n\ts_nop 0\n\tglobal_load_lds_dwordx4 %1, %2\n\ts_mov_b32 m0, %0"
                 : "=&s"(keep) : "v"(voff), "s"(sbase), "s"(lds) : "memory");
}
#define ST_AUX 16
__device__ __forceinline__ u32x4 gld(const void* p) {
    return *(const u32x4*)p;
}
__device__ __forceinline__ void lds_w128(unsigned addr, u32x4 v) {
    *(LDS_AS u32x4*)(uintptr_t)addr = v;
}

__device__ __forceinline__ void softmax16(const f32x16& p, u32x4& o0, u32x4& o1) {
    float x[16];
#pragma unroll
    for (int i = 0; i < 8; ++i) { unsigned u = pk2(p[2 * i], p[2 * i + 1]); x[2 * i] = bflo(u); x[2 * i + 1] = bfhi(u); }
    float m = fmaxf(x[0], x[1]);
#pragma unroll
    for (int i = 1; i < 8; ++i) m = fmaxf(fmaxf(m, x[2 * i]), x[2 * i + 1]);
    const float L2E = 1.4426950408889634f;
    const float mb = -m * L2E;
    float s0 = 0.f, s1 = 0.f;
#pragma unroll
    for (int i = 0; i < 8; ++i) {
        x[2 * i] = __builtin_amdgcn_exp2f(__builtin_fmaf(x[2 * i], L2E, mb)); s0 += x[2 * i];
        x[2 * i + 1] = __builtin_amdgcn_exp2f(__builtin_fmaf(x[2 * i + 1], L2E, mb)); s1 += x[2 * i + 1];
    }
    const float s = s0 + s1;
    float inv = __builtin_amdgcn_rcpf(s);
#pragma unroll
    for (int i = 0; i < 4; ++i) { o0[i] = pk2(x[2 * i] * inv, x[2 * i + 1] * inv); o1[i] = pk2(x[8 + 2 * i] * inv, x[9 + 2 * i] * inv); }
}
__device__ __forceinline__ u32x4 pack8(const f32x16& a, int o) {
    u32x4 r;
#pragma unroll
    for (int i = 0; i < 4; ++i) r[i] = pk2(a[o + 2 * i], a[o + 2 * i + 1]);
    return r;
}

__global__ __launch_bounds__(512, 2) void k1_kernel(
    const char* __restrict__ xA, const char* __restrict__ wqkv, const unsigned short* __restrict__ wphi,
    const unsigned short* __restrict__ wo, const char* __restrict__ xB, const char* __restrict__ wsre,
    const char* __restrict__ wsim, const char* __restrict__ wdre, const char* __restrict__ wdim, char* __restrict__ ws)
{
    extern __shared__ __attribute__((aligned(16))) char shm[];
    (void)shm;
    const int tid = threadIdx.x, l = tid & 63;
    const int w = __builtin_amdgcn_readfirstlane(tid >> 6);
#define IDX_XOR 0
    const int b = blockIdx.x, xcd = b & 7, idx = (b >> 3) ^ IDX_XOR;
#define MAP 1
    const int kh = xcd & 1, tm = 8 * (xcd >> 1) + (idx >> 2), tn = idx & 3;
    const int rb = 8 * (xcd >> 1) + (idx >> 2), hq = 4 * (xcd & 1) + (idx & 3);
    const int SH_R = tn, SH_AR = idx & 3, SH_AT = idx >> 2;
    constexpr int A_RL = 16, A_TL = 24;
    const int SH_T8 = idx >> 2;
#define ROTA_MODE 2
#define ROTB_MODE 0
    const int rotA = ROTA_MODE == 0 ? (rb & 7) : ROTA_MODE == 1 ? 0 : ROTA_MODE == 2 ? xcd : ((rb + hq) & 7);
    const int rotB = ROTB_MODE == 0 ? ((tm + (tm >> 4) * 5 + kh * 8) & 15) : ROTB_MODE == 1 ? ((2 * xcd + kh) & 15) : ((tm * 5 + (tm >> 4) * 3 + kh * 8) & 15);
    const int h = l >> 5, l31 = l & 31;
    const int g4 = l >> 4, q4 = (l >> 2) & 3, p4 = l & 3;
    constexpr unsigned SCR = ((LAST_A - 1) % NS) * STAGE_B;

    if (w >= 4) {
        const int lw = w - 4;
        STAMP(0, 0);
        const unsigned r8 = l >> 3, swT = (unsigned)(((l & 7) ^ (((l >> 4) & 1) << 2)) << 4);
        const unsigned swR0 = (unsigned)(((l & 7) ^ (l >> 4)) << 4), swR1 = (unsigned)(((l & 7) ^ ((l >> 4) + 4)) << 4);
        const char* aR = xA + (size_t)(64 * rb) * 1024;
        const char* aT = wqkv + (size_t)(64 * hq) * 2;
        const unsigned aRv0 = (16 * lw + r8) * 1024 + swR0, aRv1 = (16 * lw + 8 + r8) * 1024 + swR1;
        const unsigned aTv0 = (16 * lw + r8) * 3072 + swT, aTv1 = (16 * lw + 8 + r8) * 3072 + swT;
        const char* bR = (tm < 16 ? wsre : wsim) + (size_t)(128 * (tm & 15)) * 4096 + (size_t)kh * 2048;
        const char* bT = xB + (size_t)(1024 * kh) * 1024 + (size_t)(128 * tn) * 2;
        const unsigned bRv0 = (32 * lw + r8) * 4096 + swR0, bRv1 = (32 * lw + 8 + r8) * 4096 + swR1;
        const unsigned bRv2 = bRv0 + 16 * 4096, bRv3 = bRv1 + 16 * 4096;
        const unsigned bTv0 = (16 * lw + r8) * 1024 + swT, bTv1 = (16 * lw + 8 + r8) * 1024 + swT;
        auto issue = [&](int st) {
            const unsigned buf = (unsigned)(st % NS) * STAGE_B;
            if (st_isA(st)) {
                const int sa = (st_a(st) + rotA) & 7;
                const char* R = aR + sa * 128;
                const char* T = aT + (size_t)sa * (64 * 3072);
                glds16(R + aRv0, buf + (2 * lw) * 1024);
                glds16(R + aRv1, buf + (2 * lw + 1) * 1024);
#pragma unroll
                for (int u = 0; u < 3; ++u) {
                    glds16(T + u * 1024 + aTv0, buf + 8192 + u * 8192 + (2 * lw) * 1024);
                    glds16(T + u * 1024 + aTv1, buf + 8192 + u * 8192 + (2 * lw + 1) * 1024);
                }
            } else {
                const int s2 = (st_b(st) + rotB) & 15;
                const char* R = bR + s2 * 128;
                const char* T = bT + (size_t)s2 * (64 * 1024);
                glds16s(R + bRv0, buf + (4 * lw) * 1024);
                glds16s(R + bRv1, buf + (4 * lw + 1) * 1024);
                glds16s(R + bRv2, buf + (4 * lw + 2) * 1024);
                glds16s(R + bRv3, buf + (4 * lw + 3) * 1024);
#pragma unroll
                for (int u = 0; u < 2; ++u) {
                    glds16(T + u * 128 + bTv0, buf + 16384 + u * 8192 + (2 * lw) * 1024);
                    glds16(T + u * 128 + bTv1, buf + 16384 + u * 8192 + (2 * lw + 1) * 1024);
                }
            }
        };
        issue(0); issue(1);
        WAIT_VM(8);
        BARRIER();
        issue(2); issue(3);
        STAMP(0, 1);
#pragma unroll
        for (int i = 0; i <= LAST_A; ++i) {
            if (i <= NT - 4) { WAIT_VM(16); } else { WAIT_VM(8); }
            BARRIER();
            if (i + 4 < NT) issue(i + 4);
        }
        constexpr unsigned FLG = ((LAST_A - 2) % NS) * STAGE_B;
        if (l == 0) *(volatile LDS_AS unsigned*)(uintptr_t)(FLG + (unsigned)lw * 64) = 0u;
        WAIT_VM(0);
#define TPRIO 3
        __builtin_amdgcn_s_setprio(TPRIO);
        WAIT_LGKM0_BARRIER();
        STAMP(0, 3);
        const unsigned scr = SCR + (unsigned)lw * 8192;
        f32x16 kvd[2];
        {
            const u32x4 kf0 = lds_r128(scr + l * 16), kf1 = lds_r128(scr + 1024 + l * 16);
            const u32x4 wf0 = lds_r128(scr + 4096 + l * 16), wf1 = lds_r128(scr + 5120 + l * 16);
            const u32x4 vf0 = lds_r128(scr + 2048 + l * 16), vf1 = lds_r128(scr + 3072 + l * 16);
            const u32x4 ones = {0x3F803F80u, 0x3F803F80u, 0x3F803F80u, 0x3F803F80u};
            const f32x16 zero = {};
            const f32x16 pk0 = mfma32(wf0, kf0, zero), pk1 = mfma32(wf1, kf1, zero);
            u32x4 o[2][2];
            softmax16(pk0, o[0][0], o[0][1]);
            softmax16(pk1, o[1][0], o[1][1]);
            const unsigned wa = scr + l31 * 64 + h * 32;
            *(LDS_AS u32x4*)(uintptr_t)(wa + 6144) = o[0][0]; *(LDS_AS u32x4*)(uintptr_t)(wa + 6144 + 16) = o[0][1];
            *(LDS_AS u32x4*)(uintptr_t)(wa) = o[1][0]; *(LDS_AS u32x4*)(uintptr_t)(wa + 16) = o[1][1];
            asm volatile("s_waitcnt lgkmcnt(0)" ::: "memory");
#pragma unroll
            for (int hh = 0; hh < 2; ++hh) {
                const bool mine = (l31 >> 4) == hh;
                const u32x4 b0 = mine ? vf0 : ones, b1 = mine ? vf1 : ones;
                const unsigned ra = scr + (hh ? 0u : 6144u) + (unsigned)(4 * (g4 >> 1) + q4) * 64 + (unsigned)(g4 & 1) * 32 + p4 * 8;
                u32x2 a0 = lds_tr64(ra), a1 = lds_tr64(ra + 512), a2 = lds_tr64(ra + 1024), a3 = lds_tr64(ra + 1536);
                u32x4 f0 = {a0[0], a0[1], a1[0], a1[1]}, f1 = {a2[0], a2[1], a3[0], a3[1]};
                f32x16 d = mfma32(f0, b0, zero);
                kvd[hh] = mfma32(f1, b1, d);
            }
            asm volatile("s_waitcnt lgkmcnt(0)" ::: "memory");
        }
        if (lw & 1) {
#pragma unroll
            for (int hh = 0; hh < 2; ++hh)
#pragma unroll
                for (int q = 0; q < 4; ++q) {
                    f32x4 v = {kvd[hh][4 * q], kvd[hh][4 * q + 1], kvd[hh][4 * q + 2], kvd[hh][4 * q + 3]};
                    *(LDS_AS f32x4*)(uintptr_t)(scr + (hh * 4 + q) * 1024 + l * 16) = v;
                }
            asm volatile("s_waitcnt lgkmcnt(0)" ::: "memory");
            if (l == 0) *(volatile LDS_AS unsigned*)(uintptr_t)(FLG + (unsigned)lw * 64) = 1u;
            asm volatile("s_waitcnt lgkmcnt(0)" ::: "memory");
        }
        STAMP(0, 4);
        if (!(lw & 1)) {
            while (*(volatile LDS_AS unsigned*)(uintptr_t)(FLG + (unsigned)(lw + 1) * 64) == 0u) __builtin_amdgcn_s_sleep(1);
            asm volatile("" ::: "memory");
        }
        if (!(lw & 1)) {
#pragma unroll
            for (int hh = 0; hh < 2; ++hh) {
                const int head = 4 * hq + (lw & 2) + hh;
                const int nl = (l31 - 16 * hh) & 31;
                char* dst = ws + WS_KVP + ((size_t)(rb * 32 + head) * 4) * 1024 + (32 * h + nl) * 16;
#pragma unroll
                for (int q = 0; q < 4; ++q) {
                    f32x4 v = *(const LDS_AS f32x4*)(uintptr_t)(scr + 8192 + (hh * 4 + q) * 1024 + l * 16);
                    v[0] += kvd[hh][4 * q]; v[1] += kvd[hh][4 * q + 1]; v[2] += kvd[hh][4 * q + 2]; v[3] += kvd[hh][4 * q + 3];
                    if (nl <= 16) *(f32x4*)(dst + q * 1024) = v;
                }
            }
        }
        STAMP(0, 2);
        return;
    }

    STAMP(0, 0);
    const int sbk = w & 1, hp = w >> 1;
    const int fwd = b * 4 + w;
    const int gwo = b * 2 + (w & 1);
    u32x2 wda, wdc;
    unsigned short wov[8], wpv[2][8];
    {
        const int ri = fwd >> 9, eb = (fwd >> 5) & 15, db = fwd & 31;
        const char* src = (ri ? wdim : wdre) + (size_t)(32 * eb + l31) * 1024 + (16 * db + 4 * h) * 2;
        wda = *(const u32x2*)src; wdc = *(const u32x2*)(src + 16);
        const unsigned short* s2 = wo + (size_t)(16 * (gwo & 31) + 4 * h) * 512 + 32 * (gwo >> 5) + l31;
#pragma unroll
        for (int j = 0; j < 8; ++j) wov[j] = s2[((j & 3) + 8 * (j >> 2)) * 512];
        const int fr = (l31 & 3) + 4 * (l31 >> 3);
#pragma unroll
        for (int hh = 0; hh < 2; ++hh) {
            const unsigned short* s = wphi + (4 * hq + 2 * hp + hh) * 256 + (4 * h) * 16 + fr;
#pragma unroll
            for (int j = 0; j < 8; ++j) wpv[hh][j] = s[((j & 3) + 8 * (j >> 2)) * 16];
        }
        SB();
    }
    u32x4 wf[2];
    {
        const unsigned sg = (fwd >> 9) ? 0x80008000u : 0u;
        u32x4 v = {wda[0] ^ sg, wda[1] ^ sg, wdc[0] ^ sg, wdc[1] ^ sg};
        *(u32x4*)(ws + WS_WD + (size_t)fwd * 1024 + l * 16) = v;
        if (w < 2) {
            u32x4 o;
#pragma unroll
            for (int j = 0; j < 4; ++j) o[j] = (unsigned)wov[2 * j] | ((unsigned)wov[2 * j + 1] << 16);
            *(u32x4*)(ws + WS_WO + (size_t)gwo * 1024 + l * 16) = o;
        }
        if (w == 2 && l < 16) {
            const u32x4 poison = {0xFFFFFFFFu, 0xFFFFFFFFu, 0xFFFFFFFFu, 0xFFFFFFFFu};
            *(u32x4*)(ws + WS_KVZ + b * 256 + l * 16) = poison;
        }
        const unsigned sp = ((l31 >> 2) & 1) ? 0x80008000u : 0u;
#pragma unroll
        for (int hh = 0; hh < 2; ++hh)
#pragma unroll
            for (int j = 0; j < 4; ++j) wf[hh][j] = ((unsigned)wpv[hh][2 * j] | ((unsigned)wpv[hh][2 * j + 1] << 16)) ^ sp;
        SB();
    }
    const unsigned swz = (unsigned)((l31 >> 1) & 7);
    const unsigned rlane = (unsigned)l31 * 128 + ((swz ^ (unsigned)h) << 4);
    const unsigned tl = (unsigned)(8 * (g4 >> 1) + q4) * 128 + (unsigned)(2 * (g4 & 1) + (p4 >> 1)) * 16 + 8 * (p4 & 1);
    const unsigned tlane0 = tl + ((unsigned)(q4 >> 1) << 6);
    const unsigned tlane1 = tl + ((unsigned)(1 ^ (q4 >> 1)) << 6);

    f32x16 aq = {}, ak = {}, av = {}, c00 = {}, c01 = {}, c10 = {}, c11 = {};
    const int wr = w & 1, wc = w >> 1;
    unsigned rA = (unsigned)sbk * 4096 + rlane, tA = 8192 + (hp ? tlane1 : tlane0);
    unsigned rB = (unsigned)wr * 8192 + rlane;
    unsigned tB0 = 16384 + (unsigned)wc * 8192 + tlane0, tB1 = 16384 + (unsigned)wc * 8192 + tlane1;
    unsigned rX0, rX1, rX2, rX3, tX0, tX1;
#define SLOT(st) (((st) % NS) * 32768)
#define SETB(st) do { \
        asm volatile("" : "+v"(rA), "+v"(tA), "+v"(rB), "+v"(tB0), "+v"(tB1)); \
        if (st_isA(st)) { rX0 = rA + SLOT(st); tX0 = tA + SLOT(st); tX1 = tX0; } \
        else            { rX0 = rB + SLOT(st); tX0 = tB0 + SLOT(st); tX1 = tB1 + SLOT(st); } \
        rX1 = rX0 ^ 32; rX2 = rX0 ^ 64; rX3 = rX0 ^ 96; \
        asm volatile("" : "+v"(rX0), "+v"(rX1), "+v"(rX2), "+v"(rX3), "+v"(tX0), "+v"(tX1)); SB(); } while (0)
    u32x4 F00, F01, F02, F03, F10, F11, F12, F13, F20, F21, F22, F23, F30, F31, F32, F33;
#define RD01(st, k) do { \
        if (st_isA(st)) { F##k##0 = lds_r128(rX##k); F##k##1 = lds_trfrag(tX0 + k * 2048); } \
        else            { F##k##0 = lds_r128(rX##k); F##k##2 = lds_trfrag(tX0 + k * 2048); } } while (0)
#define RD23(st, k) do { \
        if (st_isA(st)) { F##k##2 = lds_trfrag(tX0 + k * 2048 + 8192); F##k##3 = lds_trfrag(tX0 + k * 2048 + 16384); } \
        else            { F##k##1 = lds_r128(rX##k + 4096); F##k##3 = lds_trfrag(tX1 + k * 2048); } } while (0)
#define STEP(st, k, nst, nk) do { \
        if (st_isA(st)) { \
            aq = mfma32(F##k##1, F##k##0, aq); SB(); \
            RD01(nst, nk); SB(); \
            ak = mfma32(F##k##2, F##k##0, ak); SB(); \
            RD23(nst, nk); SB(); \
            av = mfma32(F##k##0, F##k##3, av); SB(); \
        } else { \
            c00 = mfma32(F##k##2, F##k##0, c00); SB(); \
            RD01(nst, nk); SB(); \
            c01 = mfma32(F##k##2, F##k##1, c01); SB(); \
            RD23(nst, nk); SB(); \
            c10 = mfma32(F##k##3, F##k##0, c10); SB(); \
            c11 = mfma32(F##k##3, F##k##1, c11); SB(); \
        } } while (0)
#define STAGE(i) do { STEP(i, 0, i, 2); STEP(i, 1, i, 3); SETB((i) + 1); BARRIER(); SB(); STEP(i, 2, (i) + 1, 0); STEP(i, 3, (i) + 1, 1); } while (0)
#define STAGE3(i) do { STAGE(i); STAGE((i) + 1); STAGE((i) + 2); } while (0)
    STAMP(0, 1);
    BARRIER();
    STAMP(0, 2);
    SETB(0);
    RD01(0, 0); RD23(0, 0); RD01(0, 1); RD23(0, 1);
    SB();
    STAGE3(0); STAGE3(3); STAGE3(6); STAGE3(9); STAGE3(12); STAGE3(15); STAGE3(18);
    STAGE(21);
    {
        STAMP(0, 3);
        const unsigned scr = SCR + (unsigned)w * 8192 + l * 16;
        *(LDS_AS u32x4*)(uintptr_t)(scr) = pack8(ak, 0);
        *(LDS_AS u32x4*)(uintptr_t)(scr + 1024) = pack8(ak, 8);
        *(LDS_AS u32x4*)(uintptr_t)(scr + 2048) = pack8(av, 0);
        *(LDS_AS u32x4*)(uintptr_t)(scr + 3072) = pack8(av, 8);
        *(LDS_AS u32x4*)(uintptr_t)(scr + 4096) = wf[0];
        *(LDS_AS u32x4*)(uintptr_t)(scr + 5120) = wf[1];
        asm volatile("s_waitcnt lgkmcnt(0)\n\ts_barrier" ::: "memory");
        SB();
    }
#define STAGE_NB(i) do { STEP(i, 0, i, 2); STEP(i, 1, i, 3); SETB((i) + 1); SB(); STEP(i, 2, (i) + 1, 0); STEP(i, 3, (i) + 1, 1); } while (0)
    STAGE_NB(22); STAGE_NB(23);
    STAMP(0, 5);
    {
        const auto rs = __builtin_amdgcn_make_buffer_rsrc((void*)(ws + WS_PP), 0, 8 << 20, 0x00020000);
        const unsigned o = (unsigned)((kh * 128 + 4 * tm + 2 * wr) * 32 + 8 * tn + 4 * wc) * 1024 + l * 16;
        __builtin_amdgcn_raw_buffer_store_b128(pack8(c00, 0), rs, o, 0, ST_AUX);
        __builtin_amdgcn_raw_buffer_store_b128(pack8(c00, 8), rs, o + 1024, 0, ST_AUX);
        __builtin_amdgcn_raw_buffer_store_b128(pack8(c10, 0), rs, o + 2048, 0, ST_AUX);
        __builtin_amdgcn_raw_buffer_store_b128(pack8(c10, 8), rs, o + 3072, 0, ST_AUX);
        __builtin_amdgcn_raw_buffer_store_b128(pack8(c01, 0), rs, o + 32768, 0, ST_AUX);
        __builtin_amdgcn_raw_buffer_store_b128(pack8(c01, 8), rs, o + 32768 + 1024, 0, ST_AUX);
        __builtin_amdgcn_raw_buffer_store_b128(pack8(c11, 0), rs, o + 32768 + 2048, 0, ST_AUX);
        __builtin_amdgcn_raw_buffer_store_b128(pack8(c11, 8), rs, o + 32768 + 3072, 0, ST_AUX);
    }
    STAMP(0, 4);
    {
        const int sb = 2 * rb + sbk;
#pragma unroll
        for (int hh = 0; hh < 2; ++hh) {
            const int head = 4 * hq + 2 * hp + hh;
            const f32x16 zero = {};
            u32x4 o0, o1;
            f32x16 pq = mfma32(wf[hh], pack8(aq, 8 * hh), zero);
            softmax16(pq, o0, o1);
            const auto rs = __builtin_amdgcn_make_buffer_rsrc((void*)(ws + WS_PHIQ), 0, 4 << 20, 0x00020000);
            const unsigned o = (unsigned)((sb * 32 + head) * 2) * 1024 + l * 16;
            __builtin_amdgcn_raw_buffer_store_b128(o0, rs, o, 0, ST_AUX);
            __builtin_amdgcn_raw_buffer_store_b128(o1, rs, o + 1024, 0, ST_AUX);
        }
    }
    STAMP(0, 6);
}

constexpr int K2_LDS = NS * STAGE_B;

#define OUT_AUX 0
__device__ __forceinline__ void st_out(unsigned short* p, u32x4 v) {
    if (OUT_AUX == 2) __builtin_nontemporal_store(v, (u32x4*)p); else *(u32x4*)p = v;
}
__device__ __forceinline__ void store_tiles64(const f32x16& c0, const f32x16& c1, unsigned patch, unsigned short* gp  , int l) {
    const int h = l >> 5, l31 = l & 31;
    const unsigned wa = patch + l31 * 144 + h * 8;
#pragma unroll
    for (int q = 0; q < 4; ++q) {
        u32x2 a = {pk2(c0[4 * q], c0[4 * q + 1]), pk2(c0[4 * q + 2], c0[4 * q + 3])};
        u32x2 b = {pk2(c1[4 * q], c1[4 * q + 1]), pk2(c1[4 * q + 2], c1[4 * q + 3])};
        *(LDS_AS u32x2*)(uintptr_t)(wa + q * 16) = a;
        *(LDS_AS u32x2*)(uintptr_t)(wa + 64 + q * 16) = b;
    }
    asm volatile("s_waitcnt lgkmcnt(0)" ::: "memory");
#pragma unroll
    for (int p = 0; p < 4; ++p) {
        const int row = 8 * p + (l >> 3), ch = l & 7;
        const u32x4 v = *(const LDS_AS u32x4*)(uintptr_t)(patch + row * 144 + ch * 16);
        st_out(gp + (size_t)row * 1024 + ch * 8, v);
    }
}

__global__ __launch_bounds__(512, 2) void k2_kernel(char* __restrict__ ws, unsigned short* __restrict__ out)
{
    extern __shared__ __attribute__((aligned(16))) char shm[];
    (void)shm;
    const int tid = threadIdx.x, l = tid & 63;
    const int w = __builtin_amdgcn_readfirstlane(tid >> 6);
    const int b = blockIdx.x, xcd = b & 7, idx = b >> 3;
    const int role = idx & 1, j16 = idx >> 1;
    const int tm = 4 * xcd + (j16 >> 2), tn = j16 & 3;
    const int h = l >> 5, l31 = l & 31;
    STAMP(1, 0);
    if (role == 0) {
        if (w >= 4) {
            const int tbl = w & 1, kp = (w >> 1) & 1;
            const char* s0 = ws + WS_PP + ((size_t)((2 * tm + tbl) * 32 + 2 * kp)) * 1024 + l * 16;
            u32x4 g[4][4];
            auto load = [&](int st) {
                u32x4* d = g[st & 3];
                const size_t o = (size_t)(st >> 3) * ((size_t)64 * 32768) + (size_t)(st & 7) * 4096;
#pragma unroll
                for (int k = 0; k < 2; ++k) { d[2 * k] = *(const u32x4*)(s0 + o + k * 1024); d[2 * k + 1] = *(const u32x4*)(s0 + ((size_t)4 << 20) + o + k * 1024); }
                SB();
            };
            auto put = [&](int st) {
                const u32x4* d = g[st & 3];
                const unsigned buf = (unsigned)(st % NS) * STAGE_B + (unsigned)tbl * 4096 + (unsigned)kp * 2048 + l * 16;
#pragma unroll
                for (int k = 0; k < 2; ++k) {
                    u32x4 r;
#pragma unroll
                    for (int j = 0; j < 4; ++j) r[j] = pk2(bflo(d[2 * k][j]) + bflo(d[2 * k + 1][j]), bfhi(d[2 * k][j]) + bfhi(d[2 * k + 1][j]));
                    *(LDS_AS u32x4*)(uintptr_t)(buf + k * 1024) = r;
                }
                SB();
            };
            load(0); load(1); load(2); load(3);
            put(0);
            WAIT_LGKM0_BARRIER();
#pragma unroll
            for (int i = 0; i < 16; ++i) {
                if (i >= 1 && i + 3 < 16) load(i + 3);
                if (i + 1 < 16) put(i + 1);
                WAIT_LGKM0_BARRIER();
            }
            return;
        }
        const char* wdb = ws + WS_WD + ((size_t)((4 * tn + w) * 32)) * 1024;
        const unsigned wdv = (unsigned)l * 16;
        auto issue = [&](int st) {
            const unsigned buf = (unsigned)(st % NS) * STAGE_B + 8192 + (unsigned)w * 4096;
            const char* src = wdb + (size_t)(st >> 3) * ((size_t)16 * 32768) + (size_t)(st & 7) * 4096;
#pragma unroll
            for (int k = 0; k < 4; ++k) glds16_asm(src + k * 1024, wdv, buf + k * 1024);
            SB();
        };
        issue(0); issue(1); issue(2); issue(3);
        const int tb = w & 1, eh = w >> 1;
        f32x16 c0 = {}, c1 = {};
        unsigned yP = (unsigned)tb * 4096 + l * 16, yW = 8192 + (unsigned)eh * 8192 + l * 16;
        unsigned yp, yw;
        u32x4 P0, W00, W01, P1, W10, W11, P2, W20, W21, P3, W30, W31;
#define Y_SETB(st) do { asm volatile("" : "+v"(yP), "+v"(yW)); yp = yP + ((st) % NS) * 32768; yw = yW + ((st) % NS) * 32768; asm volatile("" : "+v"(yp), "+v"(yw)); SB(); } while (0)
#define Y_STEP(k, nk) do { \
            c0 = mfma32(W##k##0, P##k, c0); SB(); \
            P##nk = lds_r128(yp + nk * 1024); W##nk##0 = lds_r128(yw + nk * 1024); W##nk##1 = lds_r128(yw + 4096 + nk * 1024); SB(); \
            c1 = mfma32(W##k##1, P##k, c1); SB(); } while (0)
        WAIT_VM(12);
        BARRIER();
        STAMP(1, 1);
        Y_SETB(0);
        P0 = lds_r128(yp); W00 = lds_r128(yw); W01 = lds_r128(yw + 4096);
        P1 = lds_r128(yp + 1024); W10 = lds_r128(yw + 1024); W11 = lds_r128(yw + 4096 + 1024);
        SB();
#pragma unroll
        for (int i = 0; i < 16; ++i) {
            Y_STEP(0, 2); Y_STEP(1, 3);
            Y_SETB(i + 1);
            if (i <= 12) { WAIT_VM(8); } else if (i == 13) { WAIT_VM(4); } else { WAIT_VM(0); }
            BARRIER(); SB();
            if (i + 4 < 16) issue(i + 4);
            Y_STEP(2, 0); Y_STEP(3, 1);
        }
        STAMP(1, 3);
        store_tiles64(c0, c1, (13 % NS) * STAGE_B + (unsigned)w * 4608, out + (size_t)(64 * tm + 32 * tb) * 1024 + 512 + 128 * tn + 64 * eh, l);
        STAMP(1, 5);
        return;
    }
    {
        if (w < 2) {
            const int sl = (xcd * 16 + j16) * 2 + w;
            const int head = sl >> 3, q = (sl >> 1) & 3, hq = sl & 1;
            const char* src = ws + WS_KVP + ((size_t)(head * 4 + q)) * 1024 + (32 * hq + (l <= 16 ? l : 16)) * 16;
            f32x4 v[32];
#pragma unroll
            for (int r = 0; r < 32; ++r) v[r] = *(const f32x4*)(src + (size_t)r * (32 * 4096));
            f32x4 s0 = {0.f, 0.f, 0.f, 0.f}, s1 = {0.f, 0.f, 0.f, 0.f};
#pragma unroll
            for (int r = 0; r < 16; ++r) { s0 += v[2 * r]; s1 += v[2 * r + 1]; }
            s0 += s1;
            const unsigned long long pv = (unsigned long long)pk2(s0[0], s0[1]) | ((unsigned long long)pk2(s0[2], s0[3]) << 32);
            if (l < 32) {
                unsigned long long* dst = (unsigned long long*)(ws + WS_KVZ + (size_t)(head * 2 + (q & 1)) * 1024 + (l + 32 * (q >> 1)) * 16 + 8 * hq);
                __hip_atomic_store(dst, pv, __ATOMIC_RELAXED, __HIP_MEMORY_SCOPE_AGENT);
            }
            SB();
        }
        BARRIER();
        STAMP(1, 1);
        u32x4 fph[4][4], fwo[16], fkv[4][2];
        {
            const char* PH = ws + WS_PHIQ + ((size_t)((2 * tm) * 32 + 4 * w) * 2) * 1024 + l * 16;
#pragma unroll
            for (int k = 0; k < 4; ++k) {
                fph[k][0] = *(const u32x4*)(PH + k * 2048); fph[k][1] = *(const u32x4*)(PH + k * 2048 + 1024);
                fph[k][2] = *(const u32x4*)(PH + 65536 + k * 2048); fph[k][3] = *(const u32x4*)(PH + 65536 + k * 2048 + 1024);
            }
            SB();
        }
        {
            const auto rs = __builtin_amdgcn_make_buffer_rsrc((void*)(ws + WS_KVZ), 0, 65536, 0x00020000);
            const unsigned kvo = (unsigned)(4 * w) * 2048 + l * 16;
            asm volatile("s_waitcnt vmcnt(0)" ::: "memory");
            for (int spin = 0; spin < (1 << 16); ++spin) {
                bool ok = true;
#pragma unroll
                for (int k = 0; k < 4; ++k) {
                    fkv[k][0] = __builtin_amdgcn_raw_buffer_load_b128(rs, kvo + k * 2048, 0, 16);
                    fkv[k][1] = __builtin_amdgcn_raw_buffer_load_b128(rs, kvo + k * 2048 + 1024, 0, 16);
                }
#pragma unroll
                for (int k = 0; k < 4; ++k)
#pragma unroll
                    for (int i = 0; i < 4; ++i) ok = ok && fkv[k][0][i] != 0xFFFFFFFFu && fkv[k][1][i] != 0xFFFFFFFFu;
                if (__all(ok)) break;
                __builtin_amdgcn_s_sleep(8);
            }
        }
        STAMP(1, 2);
        {
            const char* WO = ws + WS_WO + ((size_t)((4 * tn + (w & 3)) * 32 + 16 * (w >> 2))) * 1024 + l * 16;
#pragma unroll
            for (int k = 0; k < 16; ++k) fwo[k] = *(const u32x4*)(WO + k * 1024);
            SB();
        }
#pragma unroll
        for (int k = 0; k < 4; ++k) {
#pragma unroll
            for (int sbi = 0; sbi < 2; ++sbi) {
                f32x16 nd = {};
                nd = mfma32(fkv[k][0], fph[k][2 * sbi], nd);
                nd = mfma32(fkv[k][1], fph[k][2 * sbi + 1], nd);
                float den = bflo(pk2(nd[8], nd[8]) & 0xffffu);
                den = bflo(pk2(den + 1e-6f, 0.f) & 0xffffu);
                const float inv = __builtin_amdgcn_rcpf(den);
                u32x4 of;
#pragma unroll
                for (int jj = 0; jj < 4; ++jj) {
                    const unsigned u = pk2(nd[2 * jj], nd[2 * jj + 1]);
                    of[jj] = pk2(bflo(u) * inv, bfhi(u) * inv);
                }
                *(LDS_AS u32x4*)(uintptr_t)((unsigned)(sbi * 32 + 4 * w + k) * 1024 + l * 16) = of;
            }
        }
        WAIT_LGKM0_BARRIER();
        STAMP(1, 3);
        f32x16 c0 = {}, c1 = {};
        {
            const unsigned ob = (unsigned)(16 * (w >> 2)) * 1024 + l * 16;
#pragma unroll
            for (int k = 0; k < 16; ++k) {
                const u32x4 o0 = lds_r128(ob + k * 1024), o1 = lds_r128(ob + 32768 + k * 1024);
                c0 = mfma32(fwo[k], o0, c0);
                c1 = mfma32(fwo[k], o1, c1);
            }
        }
        STAMP(1, 4);
        {
            const unsigned xb = 65536 + (unsigned)(w >> 2) * (64 * 528) + (unsigned)l31 * 528 + (unsigned)(w & 3) * 128 + h * 16;
#pragma unroll
            for (int q = 0; q < 4; ++q) {
                *(LDS_AS f32x4*)(uintptr_t)(xb + q * 32) = (f32x4){c0[4 * q], c0[4 * q + 1], c0[4 * q + 2], c0[4 * q + 3]};
                *(LDS_AS f32x4*)(uintptr_t)(xb + 32 * 528 + q * 32) = (f32x4){c1[4 * q], c1[4 * q + 1], c1[4 * q + 2], c1[4 * q + 3]};
            }
            WAIT_LGKM0_BARRIER();
#pragma unroll
            for (int p = 0; p < 2; ++p) {
                const int row = 8 * w + 4 * p + (l >> 4), ch = l & 15;
                const unsigned ra = 65536 + (unsigned)row * 528 + ch * 32;
                f32x4 a0 = *(const LDS_AS f32x4*)(uintptr_t)(ra), a1 = *(const LDS_AS f32x4*)(uintptr_t)(ra + 16);
                const f32x4 b0 = *(const LDS_AS f32x4*)(uintptr_t)(ra + 64 * 528), b1 = *(const LDS_AS f32x4*)(uintptr_t)(ra + 64 * 528 + 16);
                a0 += b0; a1 += b1;
                const u32x4 o = {pk2(a0[0], a0[1]), pk2(a0[2], a0[3]), pk2(a1[0], a1[1]), pk2(a1[2], a1[3])};
                st_out(out + (size_t)(64 * tm + row) * 1024 + 128 * tn + ch * 8, o);
            }
        }
        STAMP(1, 5);
    }
}

extern "C" void kernel_launch(void* const* d_in, const int* in_sizes, int n_in,
                              void* d_out, int out_size, void* d_ws, size_t ws_size,
                              hipStream_t stream) {
    (void)in_sizes; (void)n_in; (void)out_size; (void)ws_size;
    (void)hipFuncSetAttribute((const void*)k1_kernel, hipFuncAttributeMaxDynamicSharedMemorySize, K1_LDS);
    (void)hipFuncSetAttribute((const void*)k2_kernel, hipFuncAttributeMaxDynamicSharedMemorySize, K2_LDS);
    k1_kernel<<<256, 512, K1_LDS, stream>>>(
        (const char*)d_in[0], (const char*)d_in[1], (const unsigned short*)d_in[2], (const unsigned short*)d_in[3],
        (const char*)d_in[4], (const char*)d_in[5], (const char*)d_in[6], (const char*)d_in[7], (const char*)d_in[8],
        (char*)d_ws);
    k2_kernel<<<256, 512, K2_LDS, stream>>>((char*)d_ws, (unsigned short*)d_out);
}
```

```cpp
#include <hip/hip_runtime.h>
#include <stdint.h>

typedef __bf16 bf16x8 __attribute__((ext_vector_type(8)));
typedef __bf16 bf16x2 __attribute__((ext_vector_type(2)));
typedef float f32x2 __attribute__((ext_vector_type(2)));
typedef float f32x4 __attribute__((ext_vector_type(4)));
typedef float f32x16 __attribute__((ext_vector_type(16)));
typedef unsigned u32x4 __attribute__((ext_vector_type(4)));
typedef unsigned u32x2 __attribute__((ext_vector_type(2)));
typedef short s16x4 __attribute__((ext_vector_type(4)));
#define LDS_AS __attribute__((address_space(3)))
#define GLB_AS __attribute__((address_space(1)))

constexpr size_t WS_PP   = 0;
constexpr size_t WS_PHIQ = (size_t)8 << 20;
constexpr size_t WS_KVP  = (size_t)12 << 20;
constexpr size_t WS_WD   = (size_t)16 << 20;
constexpr size_t WS_WO   = (size_t)17 << 20;
constexpr size_t WS_KVZ  = ((size_t)17 << 20) + ((size_t)512 << 10);
constexpr size_t WS_END  = (size_t)18 << 20;

constexpr int NS = 5;
constexpr int STAGE_B = 32768;
constexpr int K1_LDS = NS * STAGE_B;
#define PFD 4
constexpr int NT_A = 8, NT_B = 16, NT = NT_A + NT_B;

constexpr bool st_isA(int i) { return i % 3 == 0; }
constexpr int st_a(int i) { return i / 3; }
constexpr int st_b(int i) { return (i / 3) * 2 + (i % 3) - 1; }
constexpr int LAST_A = 21;

__device__ __forceinline__ unsigned pk2(float a, float b) {
    f32x2 v = {a, b};
    return __builtin_bit_cast(unsigned, __builtin_convertvector(v, bf16x2));
}
__device__ __forceinline__ float bflo(unsigned u) { return __builtin_bit_cast(float, u << 16); }
__device__ __forceinline__ float bfhi(unsigned u) { return __builtin_bit_cast(float, u & 0xffff0000u); }
__device__ __forceinline__ f32x16 mfma32(u32x4 a, u32x4 b, f32x16 c) {
    return __builtin_amdgcn_mfma_f32_32x32x16_bf16(__builtin_bit_cast(bf16x8, a), __builtin_bit_cast(bf16x8, b), c, 0, 0, 0);
}
__device__ __forceinline__ u32x4 lds_r128(unsigned addr) {
    return *(const LDS_AS u32x4*)(uintptr_t)addr; }
__device__ __forceinline__ u32x2 lds_tr64(unsigned addr) {
    s16x4 v = __builtin_amdgcn_ds_read_tr16_b64_v4i16((LDS_AS s16x4*)(uintptr_t)addr);
    return __builtin_bit_cast(u32x2, v);
}
__device__ __forceinline__ u32x4 lds_trfrag(unsigned addr) {
    u32x2 lo = lds_tr64(addr), hi = lds_tr64(addr + 512);
    u32x4 r = {lo[0], lo[1], hi[0], hi[1]};
    return r;
}
__device__ __forceinline__ void glds16(const char* g, unsigned lds) {
    __builtin_amdgcn_global_load_lds((const GLB_AS void*)g, (LDS_AS void*)(uintptr_t)lds, 16, 0, 0);
}
#define BARRIER() asm volatile("s_barrier" ::: "memory")
#define WAIT_LGKM0_BARRIER() asm volatile("s_waitcnt lgkmcnt(0)\n\ts_barrier" ::: "memory")
#define WS_AUX 2
__device__ __forceinline__ void glds16s(const char* g, unsigned lds) {
    __builtin_amdgcn_global_load_lds((const GLB_AS void*)g, (LDS_AS void*)(uintptr_t)lds, 16, 0, WS_AUX);
}
#define WAIT_VM(n) asm volatile("s_waitcnt vmcnt(" #n ")" ::: "memory")

#define SB() __builtin_amdgcn_sched_barrier(0)
__device__ __forceinline__ void glds16_asm(const char* sbase, unsigned voff, unsigned lds) {
    unsigned keep;
    asm volatile("s_mov_b32 %0, m0\n\ts_mov_b32 m0, %3\n\ts_nop 0\n\tglobal_load_lds_dwordx4 %1, %2\n\ts_mov_b32 m0, %0"
                 : "=&s"(keep) : "v"(voff), "s"(sbase), "s"(lds) : "memory");
}
#define ST_AUX 16
__device__ __forceinline__ u32x4 gld(const void* p) {
    return *(const u32x4*)p;
}
__device__ __forceinline__ void lds_w128(unsigned addr, u32x4 v) {
    *(LDS_AS u32x4*)(uintptr_t)addr = v;
}

__device__ __forceinline__ void softmax16(const f32x16& p, u32x4& o0, u32x4& o1) {
    float x[16];
#pragma unroll
    for (int i = 0; i < 8; ++i) { unsigned u = pk2(p[2 * i], p[2 * i + 1]); x[2 * i] = bflo(u); x[2 * i + 1] = bfhi(u); }
    float m = fmaxf(x[0], x[1]);
#pragma unroll
    for (int i = 1; i < 8; ++i) m = fmaxf(fmaxf(m, x[2 * i]), x[2 * i + 1]);
    const float L2E = 1.4426950408889634f;
    const float mb = -m * L2E;
    float s0 = 0.f, s1 = 0.f;
#pragma unroll
    for (int i = 0; i < 8; ++i) {
        x[2 * i] = __builtin_amdgcn_exp2f(__builtin_fmaf(x[2 * i], L2E, mb)); s0 += x[2 * i];
        x[2 * i + 1] = __builtin_amdgcn_exp2f(__builtin_fmaf(x[2 * i + 1], L2E, mb)); s1 += x[2 * i + 1];
    }
    const float s = s0 + s1;
    float inv = __builtin_amdgcn_rcpf(s);
#pragma unroll
    for (int i = 0; i < 4; ++i) { o0[i] = pk2(x[2 * i] * inv, x[2 * i + 1] * inv); o1[i] = pk2(x[8 + 2 * i] * inv, x[9 + 2 * i] * inv); }
}
__device__ __forceinline__ u32x4 pack8(const f32x16& a, int o) {
    u32x4 r;
#pragma unroll
    for (int i = 0; i < 4; ++i) r[i] = pk2(a[o + 2 * i], a[o + 2 * i + 1]);
    return r;
}

#define PRO 2
#define SW_AUX 16
#define KV_AUX 16
template <int AUX> __device__ __forceinline__ void st_ws(char* ws, unsigned off, u32x4 v) {
    const auto rs = __builtin_amdgcn_make_buffer_rsrc((void*)ws, 0, (int)WS_END, 0x00020000);
    __builtin_amdgcn_raw_buffer_store_b128(v, rs, off, 0, AUX);
}

__global__ __launch_bounds__(512, 2) void k1_kernel(
    const char* __restrict__ xA, const char* __restrict__ wqkv, const unsigned short* __restrict__ wphi,
    const unsigned short* __restrict__ wo, const char* __restrict__ xB, const char* __restrict__ wsre,
    const char* __restrict__ wsim, const char* __restrict__ wdre, const char* __restrict__ wdim, char* __restrict__ ws)
{
    extern __shared__ __attribute__((aligned(16))) char shm[];
    (void)shm;
    const int tid = threadIdx.x, l = tid & 63;
    const int w = __builtin_amdgcn_readfirstlane(tid >> 6);
#define IDX_XOR 0
    const int b = blockIdx.x, xcd = b & 7, idx = (b >> 3) ^ IDX_XOR;
#define MAP 1
    const int kh = xcd & 1, tm = 8 * (xcd >> 1) + (idx >> 2), tn = idx & 3;
    const int rb = 8 * (xcd >> 1) + (idx >> 2), hq = 4 * (xcd & 1) + (idx & 3);
    const int SH_R = tn, SH_AR = idx & 3, SH_AT = idx >> 2;
    constexpr int A_RL = 16, A_TL = 24;
    const int SH_T8 = idx >> 2;
#define ROTA_MODE 2
#define ROTB_MODE 0
    const int rotA = ROTA_MODE == 0 ? (rb & 7) : ROTA_MODE == 1 ? 0 : ROTA_MODE == 2 ? xcd : ((rb + hq) & 7);
    const int rotB = ROTB_MODE == 0 ? ((tm + (tm >> 4) * 5 + kh * 8) & 15) : ROTB_MODE == 1 ? ((2 * xcd + kh) & 15) : ((tm * 5 + (tm >> 4) * 3 + kh * 8) & 15);
    const int h = l >> 5, l31 = l & 31;
    const int g4 = l >> 4, q4 = (l >> 2) & 3, p4 = l & 3;
    constexpr unsigned SCR = ((LAST_A - 1) % NS) * STAGE_B;

    if (w >= 4) {
        const int lw = w - 4;
        const unsigned r8 = l >> 3, swT = (unsigned)(((l & 7) ^ (((l >> 4) & 1) << 2)) << 4);
        const unsigned swR0 = (unsigned)(((l & 7) ^ (l >> 4)) << 4), swR1 = (unsigned)(((l & 7) ^ ((l >> 4) + 4)) << 4);
        const char* aR = xA + (size_t)(64 * rb) * 1024;
        const char* aT = wqkv + (size_t)(64 * hq) * 2;
        const unsigned aRv0 = (16 * lw + r8) * 1024 + swR0, aRv1 = (16 * lw + 8 + r8) * 1024 + swR1;
        const unsigned aTv0 = (16 * lw + r8) * 3072 + swT, aTv1 = (16 * lw + 8 + r8) * 3072 + swT;
        const char* bR = (tm < 16 ? wsre : wsim) + (size_t)(128 * (tm & 15)) * 4096 + (size_t)kh * 2048;
        const char* bT = xB + (size_t)(1024 * kh) * 1024 + (size_t)(128 * tn) * 2;
        const unsigned bRv0 = (32 * lw + r8) * 4096 + swR0, bRv1 = (32 * lw + 8 + r8) * 4096 + swR1;
        const unsigned bRv2 = bRv0 + 16 * 4096, bRv3 = bRv1 + 16 * 4096;
        const unsigned bTv0 = (16 * lw + r8) * 1024 + swT, bTv1 = (16 * lw + 8 + r8) * 1024 + swT;
        auto issue = [&](int st) {
            const unsigned buf = (unsigned)(st % NS) * STAGE_B;
            if (st_isA(st)) {
                const int sa = (st_a(st) + rotA) & 7;
                const char* R = aR + sa * 128;
                const char* T = aT + (size_t)sa * (64 * 3072);
                glds16(R + aRv0, buf + (2 * lw) * 1024);
                glds16(R + aRv1, buf + (2 * lw + 1) * 1024);
#pragma unroll
                for (int u = 0; u < 3; ++u) {
                    glds16(T + u * 1024 + aTv0, buf + 8192 + u * 8192 + (2 * lw) * 1024);
                    glds16(T + u * 1024 + aTv1, buf + 8192 + u * 8192 + (2 * lw + 1) * 1024);
                }
            } else {
                const int s2 = (st_b(st) + rotB) & 15;
                const char* R = bR + s2 * 128;
                const char* T = bT + (size_t)s2 * (64 * 1024);
                glds16s(R + bRv0, buf + (4 * lw) * 1024);
                glds16s(R + bRv1, buf + (4 * lw + 1) * 1024);
                glds16s(R + bRv2, buf + (4 * lw + 2) * 1024);
                glds16s(R + bRv3, buf + (4 * lw + 3) * 1024);
#pragma unroll
                for (int u = 0; u < 2; ++u) {
                    glds16(T + u * 128 + bTv0, buf + 16384 + u * 8192 + (2 * lw) * 1024);
                    glds16(T + u * 128 + bTv1, buf + 16384 + u * 8192 + (2 * lw + 1) * 1024);
                }
            }
        };
        issue(0); issue(1);
        WAIT_VM(8);
        BARRIER();
        issue(2); issue(3);
#pragma unroll
        for (int i = 0; i <= LAST_A; ++i) {
            if (i <= NT - 4) { WAIT_VM(16); } else { WAIT_VM(8); }
            BARRIER();
            if (i + 4 < NT) issue(i + 4);
        }
        constexpr unsigned FLG = ((LAST_A - 2) % NS) * STAGE_B;
        if (l == 0) *(volatile LDS_AS unsigned*)(uintptr_t)(FLG + (unsigned)lw * 64) = 0u;
        WAIT_VM(0);
#define TPRIO 3
        __builtin_amdgcn_s_setprio(TPRIO);
        WAIT_LGKM0_BARRIER();
        const unsigned scr = SCR + (unsigned)lw * 8192;
        f32x16 kvd[2];
        {
            const u32x4 kf0 = lds_r128(scr + l * 16), kf1 = lds_r128(scr + 1024 + l * 16);
            const u32x4 wf0 = lds_r128(scr + 4096 + l * 16), wf1 = lds_r128(scr + 5120 + l * 16);
            const u32x4 vf0 = lds_r128(scr + 2048 + l * 16), vf1 = lds_r128(scr + 3072 + l * 16);
            const u32x4 ones = {0x3F803F80u, 0x3F803F80u, 0x3F803F80u, 0x3F803F80u};
            const f32x16 zero = {};
            const f32x16 pk0 = mfma32(wf0, kf0, zero), pk1 = mfma32(wf1, kf1, zero);
            u32x4 o[2][2];
            softmax16(pk0, o[0][0], o[0][1]);
            softmax16(pk1, o[1][0], o[1][1]);
            const unsigned wa = scr + l31 * 64 + h * 32;
            *(LDS_AS u32x4*)(uintptr_t)(wa + 6144) = o[0][0]; *(LDS_AS u32x4*)(uintptr_t)(wa + 6144 + 16) = o[0][1];
            *(LDS_AS u32x4*)(uintptr_t)(wa) = o[1][0]; *(LDS_AS u32x4*)(uintptr_t)(wa + 16) = o[1][1];
            asm volatile("s_waitcnt lgkmcnt(0)" ::: "memory");
#pragma unroll
            for (int hh = 0; hh < 2; ++hh) {
                const bool mine = (l31 >> 4) == hh;
                const u32x4 b0 = mine ? vf0 : ones, b1 = mine ? vf1 : ones;
                const unsigned ra = scr + (hh ? 0u : 6144u) + (unsigned)(4 * (g4 >> 1) + q4) * 64 + (unsigned)(g4 & 1) * 32 + p4 * 8;
                u32x2 a0 = lds_tr64(ra), a1 = lds_tr64(ra + 512), a2 = lds_tr64(ra + 1024), a3 = lds_tr64(ra + 1536);
                u32x4 f0 = {a0[0], a0[1], a1[0], a1[1]}, f1 = {a2[0], a2[1], a3[0], a3[1]};
                f32x16 d = mfma32(f0, b0, zero);
                kvd[hh] = mfma32(f1, b1, d);
            }
            asm volatile("s_waitcnt lgkmcnt(0)" ::: "memory");
        }
        if (lw & 1) {
#pragma unroll
            for (int hh = 0; hh < 2; ++hh)
#pragma unroll
                for (int q = 0; q < 4; ++q) {
                    f32x4 v = {kvd[hh][4 * q], kvd[hh][4 * q + 1], kvd[hh][4 * q + 2], kvd[hh][4 * q + 3]};
                    *(LDS_AS f32x4*)(uintptr_t)(scr + (hh * 4 + q) * 1024 + l * 16) = v;
                }
            asm volatile("s_waitcnt lgkmcnt(0)" ::: "memory");
            if (l == 0) *(volatile LDS_AS unsigned*)(uintptr_t)(FLG + (unsigned)lw * 64) = 1u;
            asm volatile("s_waitcnt lgkmcnt(0)" ::: "memory");
        }
        if (!(lw & 1)) {
            while (*(volatile LDS_AS unsigned*)(uintptr_t)(FLG + (unsigned)(lw + 1) * 64) == 0u) __builtin_amdgcn_s_sleep(1);
            asm volatile("" ::: "memory");
        }
        if (!(lw & 1)) {
#pragma unroll
            for (int hh = 0; hh < 2; ++hh) {
                const int head = 4 * hq + (lw & 2) + hh;
                const int nl = (l31 - 16 * hh) & 31;
                const unsigned dst = (unsigned)WS_KVP + (unsigned)((rb * 32 + head) * 4) * 1024 + (unsigned)(32 * h + nl) * 16;
#pragma unroll
                for (int q = 0; q < 4; ++q) {
                    f32x4 v = *(const LDS_AS f32x4*)(uintptr_t)(scr + 8192 + (hh * 4 + q) * 1024 + l * 16);
                    v[0] += kvd[hh][4 * q]; v[1] += kvd[hh][4 * q + 1]; v[2] += kvd[hh][4 * q + 2]; v[3] += kvd[hh][4 * q + 3];
                    if (nl <= 16) st_ws<KV_AUX>(ws, dst + q * 1024, __builtin_bit_cast(u32x4, v));
                }
            }
        }
        return;
    }

    __builtin_amdgcn_s_sleep(8); SB();
    const int sbk = w & 1, hp = w >> 1;
    const int fwd = b * 4 + w;
    const int gwo = b * 2 + (w & 1);
    u32x2 wda, wdc;
    unsigned short wov[8], wpv[2][8];
    {
        const int ri = fwd >> 9, eb = (fwd >> 5) & 15, db = fwd & 31;
        const char* src = (ri ? wdim : wdre) + (size_t)(32 * eb + l31) * 1024 + (16 * db + 4 * h) * 2;
        wda = *(const u32x2*)src; wdc = *(const u32x2*)(src + 16);
        const unsigned short* s2 = wo + (size_t)(16 * (gwo & 31) + 4 * h) * 512 + 32 * (gwo >> 5) + l31;
#pragma unroll
        for (int j = 0; j < 8; ++j) wov[j] = s2[((j & 3) + 8 * (j >> 2)) * 512];
        const int fr = (l31 & 3) + 4 * (l31 >> 3);
#pragma unroll
        for (int hh = 0; hh < 2; ++hh) {
            const unsigned short* s = wphi + (4 * hq + 2 * hp + hh) * 256 + (4 * h) * 16 + fr;
#pragma unroll
            for (int j = 0; j < 8; ++j) wpv[hh][j] = s[((j & 3) + 8 * (j >> 2)) * 16];
        }
        SB();
    }
    u32x4 wf[2];
    {
        const unsigned sg = (fwd >> 9) ? 0x80008000u : 0u;
        u32x4 v = {wda[0] ^ sg, wda[1] ^ sg, wdc[0] ^ sg, wdc[1] ^ sg};
        st_ws<SW_AUX>(ws, (unsigned)WS_WD + (unsigned)fwd * 1024 + l * 16, v);
        if (w < 2) {
            u32x4 o;
#pragma unroll
            for (int j = 0; j < 4; ++j) o[j] = (unsigned)wov[2 * j] | ((unsigned)wov[2 * j + 1] << 16);
            st_ws<SW_AUX>(ws, (unsigned)WS_WO + (unsigned)gwo * 1024 + l * 16, o);
        }
        if (w == 2 && l < 16) {
            const u32x4 poison = {0xFFFFFFFFu, 0xFFFFFFFFu, 0xFFFFFFFFu, 0xFFFFFFFFu};
            st_ws<SW_AUX>(ws, (unsigned)WS_KVZ + (unsigned)b * 256 + l * 16, poison);
        }
        const unsigned sp = ((l31 >> 2) & 1) ? 0x80008000u : 0u;
#pragma unroll
        for (int hh = 0; hh < 2; ++hh)
#pragma unroll
            for (int j = 0; j < 4; ++j) wf[hh][j] = ((unsigned)wpv[hh][2 * j] | ((unsigned)wpv[hh][2 * j + 1] << 16)) ^ sp;
        SB();
    }
    const unsigned swz = (unsigned)((l31 >> 1) & 7);
    const unsigned rlane = (unsigned)l31 * 128 + ((swz ^ (unsigned)h) << 4);
    const unsigned tl = (unsigned)(8 * (g4 >> 1) + q4) * 128 + (unsigned)(2 * (g4 & 1) + (p4 >> 1)) * 16 + 8 * (p4 & 1);
    const unsigned tlane0 = tl + ((unsigned)(q4 >> 1) << 6);
    const unsigned tlane1 = tl + ((unsigned)(1 ^ (q4 >> 1)) << 6);

    f32x16 aq = {}, ak = {}, av = {}, c00 = {}, c01 = {}, c10 = {}, c11 = {};
    const int wr = w & 1, wc = w >> 1;
    unsigned rA = (unsigned)sbk * 4096 + rlane, tA = 8192 + (hp ? tlane1 : tlane0);
    unsigned rB = (unsigned)wr * 8192 + rlane;
    unsigned tB0 = 16384 + (unsigned)wc * 8192 + tlane0, tB1 = 16384 + (unsigned)wc * 8192 + tlane1;
    unsigned rX0, rX1, rX2, rX3, tX0, tX1;
#define SLOT(st) (((st) % NS) * 32768)
#define SETB(st) do { \
        asm volatile("" : "+v"(rA), "+v"(tA), "+v"(rB), "+v"(tB0), "+v"(tB1)); \
        if (st_isA(st)) { rX0 = rA + SLOT(st); tX0 = tA + SLOT(st); tX1 = tX0; } \
        else            { rX0 = rB + SLOT(st); tX0 = tB0 + SLOT(st); tX1 = tB1 + SLOT(st); } \
        rX1 = rX0 ^ 32; rX2 = rX0 ^ 64; rX3 = rX0 ^ 96; \
        asm volatile("" : "+v"(rX0), "+v"(rX1), "+v"(rX2), "+v"(rX3), "+v"(tX0), "+v"(tX1)); SB(); } while (0)
    u32x4 F00, F01, F02, F03, F10, F11, F12, F13, F20, F21, F22, F23, F30, F31, F32, F33;
#define RD01(st, k) do { \
        if (st_isA(st)) { F##k##0 = lds_r128(rX##k); F##k##1 = lds_trfrag(tX0 + k * 2048); } \
        else            { F##k##0 = lds_r128(rX##k); F##k##2 = lds_trfrag(tX0 + k * 2048); } } while (0)
#define RD23(st, k) do { \
        if (st_isA(st)) { F##k##2 = lds_trfrag(tX0 + k * 2048 + 8192); F##k##3 = lds_trfrag(tX0 + k * 2048 + 16384); } \
        else            { F##k##1 = lds_r128(rX##k + 4096); F##k##3 = lds_trfrag(tX1 + k * 2048); } } while (0)
#define STEP(st, k, nst, nk) do { \
        if (st_isA(st)) { \
            aq = mfma32(F##k##1, F##k##0, aq); SB(); \
            RD01(nst, nk); SB(); \
            ak = mfma32(F##k##2, F##k##0, ak); SB(); \
            RD23(nst, nk); SB(); \
            av = mfma32(F##k##0, F##k##3, av); SB(); \
        } else { \
            c00 = mfma32(F##k##2, F##k##0, c00); SB(); \
            RD01(nst, nk); SB(); \
            c01 = mfma32(F##k##2, F##k##1, c01); SB(); \
            RD23(nst, nk); SB(); \
            c10 = mfma32(F##k##3, F##k##0, c10); SB(); \
            c11 = mfma32(F##k##3, F##k##1, c11); SB(); \
        } } while (0)
#define STAGE(i) do { STEP(i, 0, i, 2); STEP(i, 1, i, 3); SETB((i) + 1); BARRIER(); SB(); STEP(i, 2, (i) + 1, 0); STEP(i, 3, (i) + 1, 1); } while (0)
#define STAGE3(i) do { STAGE(i); STAGE((i) + 1); STAGE((i) + 2); } while (0)
    BARRIER();
    SETB(0);
    RD01(0, 0); RD23(0, 0); RD01(0, 1); RD23(0, 1);
    SB();
    STAGE3(0); STAGE3(3); STAGE3(6); STAGE3(9); STAGE3(12); STAGE3(15); STAGE3(18);
    STAGE(21);
    {
        const unsigned scr = SCR + (unsigned)w * 8192 + l * 16;
        *(LDS_AS u32x4*)(uintptr_t)(scr) = pack8(ak, 0);
        *(LDS_AS u32x4*)(uintptr_t)(scr + 1024) = pack8(ak, 8);
        *(LDS_AS u32x4*)(uintptr_t)(scr + 2048) = pack8(av, 0);
        *(LDS_AS u32x4*)(uintptr_t)(scr + 3072) = pack8(av, 8);
        *(LDS_AS u32x4*)(uintptr_t)(scr + 4096) = wf[0];
        *(LDS_AS u32x4*)(uintptr_t)(scr + 5120) = wf[1];
        asm volatile("s_waitcnt lgkmcnt(0)\n\ts_barrier" ::: "memory");
        SB();
    }
#define STAGE_NB(i) do { STEP(i, 0, i, 2); STEP(i, 1, i, 3); SETB((i) + 1); SB(); STEP(i, 2, (i) + 1, 0); STEP(i, 3, (i) + 1, 1); } while (0)
    STAGE_NB(22); STAGE_NB(23);
    {
        const auto rs = __builtin_amdgcn_make_buffer_rsrc((void*)(ws + WS_PP), 0, 8 << 20, 0x00020000);
        const unsigned o = (unsigned)((kh * 128 + 4 * tm + 2 * wr) * 32 + 8 * tn + 4 * wc) * 1024 + l * 16;
        __builtin_amdgcn_raw_buffer_store_b128(pack8(c00, 0), rs, o, 0, ST_AUX);
        __builtin_amdgcn_raw_buffer_store_b128(pack8(c00, 8), rs, o + 1024, 0, ST_AUX);
        __builtin_amdgcn_raw_buffer_store_b128(pack8(c10, 0), rs, o + 2048, 0, ST_AUX);
        __builtin_amdgcn_raw_buffer_store_b128(pack8(c10, 8), rs, o + 3072, 0, ST_AUX);
        __builtin_amdgcn_raw_buffer_store_b128(pack8(c01, 0), rs, o + 32768, 0, ST_AUX);
        __builtin_amdgcn_raw_buffer_store_b128(pack8(c01, 8), rs, o + 32768 + 1024, 0, ST_AUX);
        __builtin_amdgcn_raw_buffer_store_b128(pack8(c11, 0), rs, o + 32768 + 2048, 0, ST_AUX);
        __builtin_amdgcn_raw_buffer_store_b128(pack8(c11, 8), rs, o + 32768 + 3072, 0, ST_AUX);
    }
    {
        const int sb = 2 * rb + sbk;
#pragma unroll
        for (int hh = 0; hh < 2; ++hh) {
            const int head = 4 * hq + 2 * hp + hh;
            const f32x16 zero = {};
            u32x4 o0, o1;
            f32x16 pq = mfma32(wf[hh], pack8(aq, 8 * hh), zero);
            softmax16(pq, o0, o1);
            const auto rs = __builtin_amdgcn_make_buffer_rsrc((void*)(ws + WS_PHIQ), 0, 4 << 20, 0x00020000);
            const unsigned o = (unsigned)((sb * 32 + head) * 2) * 1024 + l * 16;
            __builtin_amdgcn_raw_buffer_store_b128(o0, rs, o, 0, ST_AUX);
            __builtin_amdgcn_raw_buffer_store_b128(o1, rs, o + 1024, 0, ST_AUX);
        }
    }
}

constexpr int K2_LDS = NS * STAGE_B;

#define OUT_AUX 0
__device__ __forceinline__ void st_out(unsigned short* p, u32x4 v) {
    if (OUT_AUX == 2) __builtin_nontemporal_store(v, (u32x4*)p); else *(u32x4*)p = v;
}
__device__ __forceinline__ void store_tiles64(const f32x16& c0, const f32x16& c1, unsigned patch, unsigned short* gp  , int l) {
    const int h = l >> 5, l31 = l & 31;
    const unsigned wa = patch + l31 * 144 + h * 8;
#pragma unroll
    for (int q = 0; q < 4; ++q) {
        u32x2 a = {pk2(c0[4 * q], c0[4 * q + 1]), pk2(c0[4 * q + 2], c0[4 * q + 3])};
        u32x2 b = {pk2(c1[4 * q], c1[4 * q + 1]), pk2(c1[4 * q + 2], c1[4 * q + 3])};
        *(LDS_AS u32x2*)(uintptr_t)(wa + q * 16) = a;
        *(LDS_AS u32x2*)(uintptr_t)(wa + 64 + q * 16) = b;
    }
    asm volatile("s_waitcnt lgkmcnt(0)" ::: "memory");
#pragma unroll
    for (int p = 0; p < 4; ++p) {
        const int row = 8 * p + (l >> 3), ch = l & 7;
        const u32x4 v = *(const LDS_AS u32x4*)(uintptr_t)(patch + row * 144 + ch * 16);
        st_out(gp + (size_t)row * 1024 + ch * 8, v);
    }
}

__global__ __launch_bounds__(512, 2) void k2_kernel(char* __restrict__ ws, unsigned short* __restrict__ out)
{
    extern __shared__ __attribute__((aligned(16))) char shm[];
    (void)shm;
    const int tid = threadIdx.x, l = tid & 63;
    const int w = __builtin_amdgcn_readfirstlane(tid >> 6);
    const int b = blockIdx.x, xcd = b & 7, idx = b >> 3;
    const int role = idx & 1, j16 = idx >> 1;
    const int tm = 4 * xcd + (j16 >> 2), tn = j16 & 3;
    const int h = l >> 5, l31 = l & 31;
    if (role == 0) {
        if (w >= 4) {
            const int tbl = w & 1, kp = (w >> 1) & 1;
            const char* s0 = ws + WS_PP + ((size_t)((2 * tm + tbl) * 32 + 2 * kp)) * 1024 + l * 16;
            u32x4 g[4][4];
            auto load = [&](int st) {
                u32x4* d = g[st & 3];
                const size_t o = (size_t)(st >> 3) * ((size_t)64 * 32768) + (size_t)(st & 7) * 4096;
#pragma unroll
                for (int k = 0; k < 2; ++k) { d[2 * k] = *(const u32x4*)(s0 + o + k * 1024); d[2 * k + 1] = *(const u32x4*)(s0 + ((size_t)4 << 20) + o + k * 1024); }
                SB();
            };
            auto put = [&](int st) {
                const u32x4* d = g[st & 3];
                const unsigned buf = (unsigned)(st % NS) * STAGE_B + (unsigned)tbl * 4096 + (unsigned)kp * 2048 + l * 16;
#pragma unroll
                for (int k = 0; k < 2; ++k) {
                    u32x4 r;
#pragma unroll
                    for (int j = 0; j < 4; ++j) r[j] = pk2(bflo(d[2 * k][j]) + bflo(d[2 * k + 1][j]), bfhi(d[2 * k][j]) + bfhi(d[2 * k + 1][j]));
                    *(LDS_AS u32x4*)(uintptr_t)(buf + k * 1024) = r;
                }
                SB();
            };
            load(0); load(1); load(2); load(3);
            put(0);
            WAIT_LGKM0_BARRIER();
#pragma unroll
            for (int i = 0; i < 16; ++i) {
                if (i >= 1 && i + 3 < 16) load(i + 3);
                if (i + 1 < 16) put(i + 1);
                WAIT_LGKM0_BARRIER();
            }
            return;
        }
        const char* wdb = ws + WS_WD + ((size_t)((4 * tn + w) * 32)) * 1024;
        const unsigned wdv = (unsigned)l * 16;
        auto issue = [&](int st) {
            const unsigned buf = (unsigned)(st % NS) * STAGE_B + 8192 + (unsigned)w * 4096;
            const char* src = wdb + (size_t)(st >> 3) * ((size_t)16 * 32768) + (size_t)(st & 7) * 4096;
#pragma unroll
            for (int k = 0; k < 4; ++k) glds16_asm(src + k * 1024, wdv, buf + k * 1024);
            SB();
        };
        issue(0); issue(1); issue(2); issue(3);
        const int tb = w & 1, eh = w >> 1;
        f32x16 c0 = {}, c1 = {};
        unsigned yP = (unsigned)tb * 4096 + l * 16, yW = 8192 + (unsigned)eh * 8192 + l * 16;
        unsigned yp, yw;
        u32x4 P0, W00, W01, P1, W10, W11, P2, W20, W21, P3, W30, W31;
#define Y_SETB(st) do { asm volatile("" : "+v"(yP), "+v"(yW)); yp = yP + ((st) % NS) * 32768; yw = yW + ((st) % NS) * 32768; asm volatile("" : "+v"(yp), "+v"(yw)); SB(); } while (0)
#define Y_STEP(k, nk) do { \
            c0 = mfma32(W##k##0, P##k, c0); SB(); \
            P##nk = lds_r128(yp + nk * 1024); W##nk##0 = lds_r128(yw + nk * 1024); W##nk##1 = lds_r128(yw + 4096 + nk * 1024); SB(); \
            c1 = mfma32(W##k##1, P##k, c1); SB(); } while (0)
        WAIT_VM(12);
        BARRIER();
        Y_SETB(0);
        P0 = lds_r128(yp); W00 = lds_r128(yw); W01 = lds_r128(yw + 4096);
        P1 = lds_r128(yp + 1024); W10 = lds_r128(yw + 1024); W11 = lds_r128(yw + 4096 + 1024);
        SB();
#pragma unroll
        for (int i = 0; i < 16; ++i) {
            Y_STEP(0, 2); Y_STEP(1, 3);
            Y_SETB(i + 1);
            if (i <= 12) { WAIT_VM(8); } else if (i == 13) { WAIT_VM(4); } else { WAIT_VM(0); }
            BARRIER(); SB();
            if (i + 4 < 16) issue(i + 4);
            Y_STEP(2, 0); Y_STEP(3, 1);
        }
        store_tiles64(c0, c1, (13 % NS) * STAGE_B + (unsigned)w * 4608, out + (size_t)(64 * tm + 32 * tb) * 1024 + 512 + 128 * tn + 64 * eh, l);
        return;
    }
    {
        if (w < 2) {
            const int sl = (xcd * 16 + j16) * 2 + w;
            const int head = sl >> 3, q = (sl >> 1) & 3, hq = sl & 1;
            const char* src = ws + WS_KVP + ((size_t)(head * 4 + q)) * 1024 + (32 * hq + (l <= 16 ? l : 16)) * 16;
            f32x4 v[32];
#pragma unroll
            for (int r = 0; r < 32; ++r) v[r] = *(const f32x4*)(src + (size_t)r * (32 * 4096));
            f32x4 s0 = {0.f, 0.f, 0.f, 0.f}, s1 = {0.f, 0.f, 0.f, 0.f};
#pragma unroll
            for (int r = 0; r < 16; ++r) { s0 += v[2 * r]; s1 += v[2 * r + 1]; }
            s0 += s1;
            const unsigned long long pv = (unsigned long long)pk2(s0[0], s0[1]) | ((unsigned long long)pk2(s0[2], s0[3]) << 32);
            if (l < 32) {
                unsigned long long* dst = (unsigned long long*)(ws + WS_KVZ + (size_t)(head * 2 + (q & 1)) * 1024 + (l + 32 * (q >> 1)) * 16 + 8 * hq);
                __hip_atomic_store(dst, pv, __ATOMIC_RELAXED, __HIP_MEMORY_SCOPE_AGENT);
            }
            SB();
        }
        BARRIER();
        u32x4 fph[4][4], fwo[16], fkv[4][2];
        {
            const char* PH = ws + WS_PHIQ + ((size_t)((2 * tm) * 32 + 4 * w) * 2) * 1024 + l * 16;
#pragma unroll
            for (int k = 0; k < 4; ++k) {
                fph[k][0] = *(const u32x4*)(PH + k * 2048); fph[k][1] = *(const u32x4*)(PH + k * 2048 + 1024);
                fph[k][2] = *(const u32x4*)(PH + 65536 + k * 2048); fph[k][3] = *(const u32x4*)(PH + 65536 + k * 2048 + 1024);
            }
            SB();
        }
        {
            const auto rs = __builtin_amdgcn_make_buffer_rsrc((void*)(ws + WS_KVZ), 0, 65536, 0x00020000);
            const unsigned kvo = (unsigned)(4 * w) * 2048 + l * 16;
            asm volatile("s_waitcnt vmcnt(0)" ::: "memory");
            for (int spin = 0; spin < (1 << 16); ++spin) {
                bool ok = true;
#pragma unroll
                for (int k = 0; k < 4; ++k) {
                    fkv[k][0] = __builtin_amdgcn_raw_buffer_load_b128(rs, kvo + k * 2048, 0, 16);
                    fkv[k][1] = __builtin_amdgcn_raw_buffer_load_b128(rs, kvo + k * 2048 + 1024, 0, 16);
                }
#pragma unroll
                for (int k = 0; k < 4; ++k)
#pragma unroll
                    for (int i = 0; i < 4; ++i) ok = ok && fkv[k][0][i] != 0xFFFFFFFFu && fkv[k][1][i] != 0xFFFFFFFFu;
                if (__all(ok)) break;
                __builtin_amdgcn_s_sleep(8);
            }
        }
        {
            const char* WO = ws + WS_WO + ((size_t)((4 * tn + (w & 3)) * 32 + 16 * (w >> 2))) * 1024 + l * 16;
#pragma unroll
            for (int k = 0; k < 16; ++k) fwo[k] = *(const u32x4*)(WO + k * 1024);
            SB();
        }
#pragma unroll
        for (int k = 0; k < 4; ++k) {
#pragma unroll
            for (int sbi = 0; sbi < 2; ++sbi) {
                f32x16 nd = {};
                nd = mfma32(fkv[k][0], fph[k][2 * sbi], nd);
                nd = mfma32(fkv[k][1], fph[k][2 * sbi + 1], nd);
                float den = bflo(pk2(nd[8], nd[8]) & 0xffffu);
                den = bflo(pk2(den + 1e-6f, 0.f) & 0xffffu);
                const float inv = __builtin_amdgcn_rcpf(den);
                u32x4 of;
#pragma unroll
                for (int jj = 0; jj < 4; ++jj) {
                    const unsigned u = pk2(nd[2 * jj], nd[2 * jj + 1]);
                    of[jj] = pk2(bflo(u) * inv, bfhi(u) * inv);
                }
                *(LDS_AS u32x4*)(uintptr_t)((unsigned)(sbi * 32 + 4 * w + k) * 1024 + l * 16) = of;
            }
        }
        WAIT_LGKM0_BARRIER();
        f32x16 c0 = {}, c1 = {};
        {
            const unsigned ob = (unsigned)(16 * (w >> 2)) * 1024 + l * 16;
#pragma unroll
            for (int k = 0; k < 16; ++k) {
                const u32x4 o0 = lds_r128(ob + k * 1024), o1 = lds_r128(ob + 32768 + k * 1024);
                c0 = mfma32(fwo[k], o0, c0);
                c1 = mfma32(fwo[k], o1, c1);
            }
        }
        {
            const unsigned xb = 65536 + (unsigned)(w >> 2) * (64 * 528) + (unsigned)l31 * 528 + (unsigned)(w & 3) * 128 + h * 16;
#pragma unroll
            for (int q = 0; q < 4; ++q) {
                *(LDS_AS f32x4*)(uintptr_t)(xb + q * 32) = (f32x4){c0[4 * q], c0[4 * q + 1], c0[4 * q + 2], c0[4 * q + 3]};
                *(LDS_AS f32x4*)(uintptr_t)(xb + 32 * 528 + q * 32) = (f32x4){c1[4 * q], c1[4 * q + 1], c1[4 * q + 2], c1[4 * q + 3]};
            }
            WAIT_LGKM0_BARRIER();
#pragma unroll
            for (int p = 0; p < 2; ++p) {
                const int row = 8 * w + 4 * p + (l >> 4), ch = l & 15;
                const unsigned ra = 65536 + (unsigned)row * 528 + ch * 32;
                f32x4 a0 = *(const LDS_AS f32x4*)(uintptr_t)(ra), a1 = *(const LDS_AS f32x4*)(uintptr_t)(ra + 16);
                const f32x4 b0 = *(const LDS_AS f32x4*)(uintptr_t)(ra + 64 * 528), b1 = *(const LDS_AS f32x4*)(uintptr_t)(ra + 64 * 528 + 16);
                a0 += b0; a1 += b1;
                const u32x4 o = {pk2(a0[0], a0[1]), pk2(a0[2], a0[3]), pk2(a1[0], a1[1]), pk2(a1[2], a1[3])};
                st_out(out + (size_t)(64 * tm + row) * 1024 + 128 * tn + ch * 8, o);
            }
        }
    }
}

extern "C" void kernel_launch(void* const* d_in, const int* in_sizes, int n_in,
                              void* d_out, int out_size, void* d_ws, size_t ws_size,
                              hipStream_t stream) {
    (void)in_sizes; (void)n_in; (void)out_size; (void)ws_size;
    (void)hipFuncSetAttribute((const void*)k1_kernel, hipFuncAttributeMaxDynamicSharedMemorySize, K1_LDS);
    (void)hipFuncSetAttribute((const void*)k2_kernel, hipFuncAttributeMaxDynamicSharedMemorySize, K2_LDS);
    k1_kernel<<<256, 512, K1_LDS, stream>>>(
        (const char*)d_in[0], (const char*)d_in[1], (const unsigned short*)d_in[2], (const unsigned short*)d_in[3],
        (const char*)d_in[4], (const char*)d_in[5], (const char*)d_in[6], (const char*)d_in[7], (const char*)d_in[8],
        (char*)d_ws);
    k2_kernel<<<256, 512, K2_LDS, stream>>>((char*)d_ws, (unsigned short*)d_out);
}
```

```cpp
#include <hip/hip_runtime.h>
#include <stdint.h>

typedef __bf16 bf16x8 __attribute__((ext_vector_type(8)));
typedef __bf16 bf16x2 __attribute__((ext_vector_type(2)));
typedef float f32x2 __attribute__((ext_vector_type(2)));
typedef float f32x4 __attribute__((ext_vector_type(4)));
typedef float f32x16 __attribute__((ext_vector_type(16)));
typedef unsigned u32x4 __attribute__((ext_vector_type(4)));
typedef unsigned u32x2 __attribute__((ext_vector_type(2)));
typedef short s16x4 __attribute__((ext_vector_type(4)));
#define LDS_AS __attribute__((address_space(3)))
#define GLB_AS __attribute__((address_space(1)))

constexpr size_t WS_PP   = 0;
constexpr size_t WS_PHIQ = (size_t)8 << 20;
constexpr size_t WS_KVP  = (size_t)12 << 20;
constexpr size_t WS_WD   = (size_t)16 << 20;
constexpr size_t WS_WO   = (size_t)17 << 20;
constexpr size_t WS_KVZ  = ((size_t)17 << 20) + ((size_t)512 << 10);
constexpr size_t WS_END  = (size_t)18 << 20;

constexpr int NS = 5;
constexpr int STAGE_B = 32768;
constexpr int K1_LDS = NS * STAGE_B;
#define PFD 4
constexpr int NT_A = 8, NT_B = 16, NT = NT_A + NT_B;

constexpr bool st_isA(int i) { return i % 3 == 0; }
constexpr int st_a(int i) { return i / 3; }
constexpr int st_b(int i) { return (i / 3) * 2 + (i % 3) - 1; }
constexpr int LAST_A = 21;

__device__ __forceinline__ unsigned pk2(float a, float b) {
    f32x2 v = {a, b};
    return __builtin_bit_cast(unsigned, __builtin_convertvector(v, bf16x2));
}
__device__ __forceinline__ float bflo(unsigned u) { return __builtin_bit_cast(float, u << 16); }
__device__ __forceinline__ float bfhi(unsigned u) { return __builtin_bit_cast(float, u & 0xffff0000u); }
__device__ __forceinline__ f32x16 mfma32(u32x4 a, u32x4 b, f32x16 c) {
    return __builtin_amdgcn_mfma_f32_32x32x16_bf16(__builtin_bit_cast(bf16x8, a), __builtin_bit_cast(bf16x8, b), c, 0, 0, 0);
}
__device__ __forceinline__ u32x4 lds_r128(unsigned addr) {
    return *(const LDS_AS u32x4*)(uintptr_t)addr; }
__device__ __forceinline__ u32x2 lds_tr64(unsigned addr) {
    s16x4 v = __builtin_amdgcn_ds_read_tr16_b64_v4i16((LDS_AS s16x4*)(uintptr_t)addr);
    return __builtin_bit_cast(u32x2, v);
}
__device__ __forceinline__ u32x4 lds_trfrag(unsigned addr) {
    u32x2 lo = lds_tr64(addr), hi = lds_tr64(addr + 512);
    u32x4 r = {lo[0], lo[1], hi[0], hi[1]};
    return r;
}
__device__ __forceinline__ void glds16(const char* g, unsigned lds) {
    __builtin_amdgcn_global_load_lds((const GLB_AS void*)g, (LDS_AS void*)(uintptr_t)lds, 16, 0, 0);
}
#define BARRIER() asm volatile("s_barrier" ::: "memory")
#define WAIT_LGKM0_BARRIER() asm volatile("s_waitcnt lgkmcnt(0)\n\ts_barrier" ::: "memory")
#define WS_AUX 2
__device__ __forceinline__ void glds16s(const char* g, unsigned lds) {
    __builtin_amdgcn_global_load_lds((const GLB_AS void*)g, (LDS_AS void*)(uintptr_t)lds, 16, 0, WS_AUX);
}
#define WAIT_VM(n) asm volatile("s_waitcnt vmcnt(" #n ")" ::: "memory")

#define SB() __builtin_amdgcn_sched_barrier(0)
__device__ __forceinline__ void glds16_asm(const char* sbase, unsigned voff, unsigned lds) {
    unsigned keep;
    asm volatile("s_mov_b32 %0, m0\n\ts_mov_b32 m0, %3\n\ts_nop 0\n\tglobal_load_lds_dwordx4 %1, %2\n\ts_mov_b32 m0, %0"
                 : "=&s"(keep) : "v"(voff), "s"(sbase), "s"(lds) : "memory");
}
#define ST_AUX 16
__device__ __forceinline__ u32x4 gld(const void* p) {
    return *(const u32x4*)p;
}
__device__ __forceinline__ void lds_w128(unsigned addr, u32x4 v) {
    *(LDS_AS u32x4*)(uintptr_t)addr = v;
}

__device__ __forceinline__ void softmax16(const f32x16& p, u32x4& o0, u32x4& o1) {
    float x[16];
#pragma unroll
    for (int i = 0; i < 8; ++i) { unsigned u = pk2(p[2 * i], p[2 * i + 1]); x[2 * i] = bflo(u); x[2 * i + 1] = bfhi(u); }
    float m = fmaxf(x[0], x[1]);
#pragma unroll
    for (int i = 1; i < 8; ++i) m = fmaxf(fmaxf(m, x[2 * i]), x[2 * i + 1]);
    const float L2E = 1.4426950408889634f;
    const float mb = -m * L2E;
    float s0 = 0.f, s1 = 0.f;
#pragma unroll
    for (int i = 0; i < 8; ++i) {
        x[2 * i] = __builtin_amdgcn_exp2f(__builtin_fmaf(x[2 * i], L2E, mb)); s0 += x[2 * i];
        x[2 * i + 1] = __builtin_amdgcn_exp2f(__builtin_fmaf(x[2 * i + 1], L2E, mb)); s1 += x[2 * i + 1];
    }
    const float s = s0 + s1;
    float inv = __builtin_amdgcn_rcpf(s);
#pragma unroll
    for (int i = 0; i < 4; ++i) { o0[i] = pk2(x[2 * i] * inv, x[2 * i + 1] * inv); o1[i] = pk2(x[8 + 2 * i] * inv, x[9 + 2 * i] * inv); }
}
__device__ __forceinline__ u32x4 pack8(const f32x16& a, int o) {
    u32x4 r;
#pragma unroll
    for (int i = 0; i < 4; ++i) r[i] = pk2(a[o + 2 * i], a[o + 2 * i + 1]);
    return r;
}

#define PRO 2
#define SW_AUX 16
#define KV_AUX 16
template <int AUX> __device__ __forceinline__ void st_ws(char* ws, unsigned off, u32x4 v) {
    const auto rs = __builtin_amdgcn_make_buffer_rsrc((void*)ws, 0, (int)WS_END, 0x00020000);
    __builtin_amdgcn_raw_buffer_store_b128(v, rs, off, 0, AUX);
}

__global__ __launch_bounds__(512, 2) void k1_kernel(
    const char* __restrict__ xA, const char* __restrict__ wqkv, const unsigned short* __restrict__ wphi,
    const unsigned short* __restrict__ wo, const char* __restrict__ xB, const char* __restrict__ wsre,
    const char* __restrict__ wsim, const char* __restrict__ wdre, const char* __restrict__ wdim, char* __restrict__ ws)
{
    extern __shared__ __attribute__((aligned(16))) char shm[];
    (void)shm;
    const int tid = threadIdx.x, l = tid & 63;
    const int w = __builtin_amdgcn_readfirstlane(tid >> 6);
#define IDX_XOR 0
    const int b = blockIdx.x, xcd = b & 7, idx = (b >> 3) ^ IDX_XOR;
#define MAP 1
    const int kh = xcd & 1, tm = 8 * (xcd >> 1) + (idx >> 2), tn = idx & 3;
    const int rb = 8 * (xcd >> 1) + (idx >> 2), hq = 4 * (xcd & 1) + (idx & 3);
    const int SH_R = tn, SH_AR = idx & 3, SH_AT = idx >> 2;
    constexpr int A_RL = 16, A_TL = 24;
    const int SH_T8 = idx >> 2;
#define ROTA_MODE 2
#define ROTB_MODE 0
    const int rotA = ROTA_MODE == 0 ? (rb & 7) : ROTA_MODE == 1 ? 0 : ROTA_MODE == 2 ? xcd : ((rb + hq) & 7);
    const int rotB = ROTB_MODE == 0 ? ((tm + (tm >> 4) * 5 + kh * 8) & 15) : ROTB_MODE == 1 ? ((2 * xcd + kh) & 15) : ((tm * 5 + (tm >> 4) * 3 + kh * 8) & 15);
    const int h = l >> 5, l31 = l & 31;
    const int g4 = l >> 4, q4 = (l >> 2) & 3, p4 = l & 3;
    constexpr unsigned SCR = ((LAST_A - 1) % NS) * STAGE_B;

    if (w >= 4) {
        const int lw = w - 4;
        const unsigned r8 = l >> 3, swT = (unsigned)(((l & 7) ^ (((l >> 4) & 1) << 2)) << 4);
        const unsigned swR0 = (unsigned)(((l & 7) ^ (l >> 4)) << 4), swR1 = (unsigned)(((l & 7) ^ ((l >> 4) + 4)) << 4);
        const char* aR = xA + (size_t)(64 * rb) * 1024;
        const char* aT = wqkv + (size_t)(64 * hq) * 2;
        const unsigned aRv0 = (16 * lw + r8) * 1024 + swR0, aRv1 = (16 * lw + 8 + r8) * 1024 + swR1;
        const unsigned aTv0 = (16 * lw + r8) * 3072 + swT, aTv1 = (16 * lw + 8 + r8) * 3072 + swT;
        const char* bR = (tm < 16 ? wsre : wsim) + (size_t)(128 * (tm & 15)) * 4096 + (size_t)kh * 2048;
        const char* bT = xB + (size_t)(1024 * kh) * 1024 + (size_t)(128 * tn) * 2;
        const unsigned bRv0 = (32 * lw + r8) * 4096 + swR0, bRv1 = (32 * lw + 8 + r8) * 4096 + swR1;
        const unsigned bRv2 = bRv0 + 16 * 4096, bRv3 = bRv1 + 16 * 4096;
        const unsigned bTv0 = (16 * lw + r8) * 1024 + swT, bTv1 = (16 * lw + 8 + r8) * 1024 + swT;
        auto issue = [&](int st) {
            const unsigned buf = (unsigned)(st % NS) * STAGE_B;
            if (st_isA(st)) {
                const int sa = (st_a(st) + rotA) & 7;
                const char* R = aR + sa * 128;
                const char* T = aT + (size_t)sa * (64 * 3072);
                glds16(R + aRv0, buf + (2 * lw) * 1024);
                glds16(R + aRv1, buf + (2 * lw + 1) * 1024);
#pragma unroll
                for (int u = 0; u < 3; ++u) {
                    glds16(T + u * 1024 + aTv0, buf + 8192 + u * 8192 + (2 * lw) * 1024);
                    glds16(T + u * 1024 + aTv1, buf + 8192 + u * 8192 + (2 * lw + 1) * 1024);
                }
            } else {
                const int s2 = (st_b(st) + rotB) & 15;
                const char* R = bR + s2 * 128;
                const char* T = bT + (size_t)s2 * (64 * 1024);
                glds16s(R + bRv0, buf + (4 * lw) * 1024);
                glds16s(R + bRv1, buf + (4 * lw + 1) * 1024);
                glds16s(R + bRv2, buf + (4 * lw + 2) * 1024);
                glds16s(R + bRv3, buf + (4 * lw + 3) * 1024);
#pragma unroll
                for (int u = 0; u < 2; ++u) {
                    glds16(T + u * 128 + bTv0, buf + 16384 + u * 8192 + (2 * lw) * 1024);
                    glds16(T + u * 128 + bTv1, buf + 16384 + u * 8192 + (2 * lw + 1) * 1024);
                }
            }
        };
        issue(0); issue(1);
        WAIT_VM(8);
        BARRIER();
        issue(2); issue(3);
#pragma unroll
        for (int i = 0; i <= LAST_A; ++i) {
            if (i <= NT - 4) { WAIT_VM(16); } else { WAIT_VM(8); }
            BARRIER();
            if (i + 4 < NT) issue(i + 4);
        }
        constexpr unsigned FLG = ((LAST_A - 2) % NS) * STAGE_B;
        if (l == 0) *(volatile LDS_AS unsigned*)(uintptr_t)(FLG + (unsigned)lw * 64) = 0u;
        WAIT_VM(0);
#define TPRIO 3
        __builtin_amdgcn_s_setprio(TPRIO);
        WAIT_LGKM0_BARRIER();
        const unsigned scr = SCR + (unsigned)lw * 8192;
        f32x16 kvd[2];
        {
            const u32x4 kf0 = lds_r128(scr + l * 16), kf1 = lds_r128(scr + 1024 + l * 16);
            const u32x4 wf0 = lds_r128(scr + 4096 + l * 16), wf1 = lds_r128(scr + 5120 + l * 16);
            const u32x4 vf0 = lds_r128(scr + 2048 + l * 16), vf1 = lds_r128(scr + 3072 + l * 16);
            const u32x4 ones = {0x3F803F80u, 0x3F803F80u, 0x3F803F80u, 0x3F803F80u};
            const f32x16 zero = {};
            const f32x16 pk0 = mfma32(wf0, kf0, zero), pk1 = mfma32(wf1, kf1, zero);
            u32x4 o[2][2];
            softmax16(pk0, o[0][0], o[0][1]);
            softmax16(pk1, o[1][0], o[1][1]);
            const unsigned wa = scr + l31 * 64 + h * 32;
            *(LDS_AS u32x4*)(uintptr_t)(wa + 6144) = o[0][0]; *(LDS_AS u32x4*)(uintptr_t)(wa + 6144 + 16) = o[0][1];
            *(LDS_AS u32x4*)(uintptr_t)(wa) = o[1][0]; *(LDS_AS u32x4*)(uintptr_t)(wa + 16) = o[1][1];
            asm volatile("s_waitcnt lgkmcnt(0)" ::: "memory");
#pragma unroll
            for (int hh = 0; hh < 2; ++hh) {
                const bool mine = (l31 >> 4) == hh;
                const u32x4 b0 = mine ? vf0 : ones, b1 = mine ? vf1 : ones;
                const unsigned ra = scr + (hh ? 0u : 6144u) + (unsigned)(4 * (g4 >> 1) + q4) * 64 + (unsigned)(g4 & 1) * 32 + p4 * 8;
                u32x2 a0 = lds_tr64(ra), a1 = lds_tr64(ra + 512), a2 = lds_tr64(ra + 1024), a3 = lds_tr64(ra + 1536);
                u32x4 f0 = {a0[0], a0[1], a1[0], a1[1]}, f1 = {a2[0], a2[1], a3[0], a3[1]};
                f32x16 d = mfma32(f0, b0, zero);
                kvd[hh] = mfma32(f1, b1, d);
            }
            asm volatile("s_waitcnt lgkmcnt(0)" ::: "memory");
        }
        if (lw & 1) {
#pragma unroll
            for (int hh = 0; hh < 2; ++hh)
#pragma unroll
                for (int q = 0; q < 4; ++q) {
                    f32x4 v = {kvd[hh][4 * q], kvd[hh][4 * q + 1], kvd[hh][4 * q + 2], kvd[hh][4 * q + 3]};
                    *(LDS_AS f32x4*)(uintptr_t)(scr + (hh * 4 + q) * 1024 + l * 16) = v;
                }
            asm volatile("s_waitcnt lgkmcnt(0)" ::: "memory");
            if (l == 0) *(volatile LDS_AS unsigned*)(uintptr_t)(FLG + (unsigned)lw * 64) = 1u;
            asm volatile("s_waitcnt lgkmcnt(0)" ::: "memory");
        }
        if (!(lw & 1)) {
            while (*(volatile LDS_AS unsigned*)(uintptr_t)(FLG + (unsigned)(lw + 1) * 64) == 0u) __builtin_amdgcn_s_sleep(1);
            asm volatile("" ::: "memory");
        }
        if (!(lw & 1)) {
#pragma unroll
            for (int hh = 0; hh < 2; ++hh) {
                const int head = 4 * hq + (lw & 2) + hh;
                const int nl = (l31 - 16 * hh) & 31;
                const unsigned dst = (unsigned)WS_KVP + (unsigned)((rb * 32 + head) * 4) * 1024 + (unsigned)(32 * h + nl) * 16;
#pragma unroll
                for (int q = 0; q < 4; ++q) {
                    f32x4 v = *(const LDS_AS f32x4*)(uintptr_t)(scr + 8192 + (hh * 4 + q) * 1024 + l * 16);
                    v[0] += kvd[hh][4 * q]; v[1] += kvd[hh][4 * q + 1]; v[2] += kvd[hh][4 * q + 2]; v[3] += kvd[hh][4 * q + 3];
                    if (nl <= 16) st_ws<KV_AUX>(ws, dst + q * 1024, __builtin_bit_cast(u32x4, v));
                }
            }
        }
        return;
    }

    __builtin_amdgcn_s_sleep(8); SB();
    const int sbk = w & 1, hp = w >> 1;
    const int fwd = b * 4 + w;
    const int gwo = b * 2 + (w & 1);
    u32x2 wda, wdc;
    unsigned short wov[8], wpv[2][8];
    {
        const int ri = fwd >> 9, eb = (fwd >> 5) & 15, db = fwd & 31;
        const char* src = (ri ? wdim : wdre) + (size_t)(32 * eb + l31) * 1024 + (16 * db + 4 * h) * 2;
        wda = *(const u32x2*)src; wdc = *(const u32x2*)(src + 16);
        const unsigned short* s2 = wo + (size_t)(16 * (gwo & 31) + 4 * h) * 512 + 32 * (gwo >> 5) + l31;
#pragma unroll
        for (int j = 0; j < 8; ++j) wov[j] = s2[((j & 3) + 8 * (j >> 2)) * 512];
        const int fr = (l31 & 3) + 4 * (l31 >> 3);
#pragma unroll
        for (int hh = 0; hh < 2; ++hh) {
            const unsigned short* s = wphi + (4 * hq + 2 * hp + hh) * 256 + (4 * h) * 16 + fr;
#pragma unroll
            for (int j = 0; j < 8; ++j) wpv[hh][j] = s[((j & 3) + 8 * (j >> 2)) * 16];
        }
        SB();
    }
    u32x4 wf[2];
    {
        const unsigned sg = (fwd >> 9) ? 0x80008000u : 0u;
        u32x4 v = {wda[0] ^ sg, wda[1] ^ sg, wdc[0] ^ sg, wdc[1] ^ sg};
        st_ws<SW_AUX>(ws, (unsigned)WS_WD + (unsigned)fwd * 1024 + l * 16, v);
        if (w < 2) {
            u32x4 o;
#pragma unroll
            for (int j = 0; j < 4; ++j) o[j] = (unsigned)wov[2 * j] | ((unsigned)wov[2 * j + 1] << 16);
            st_ws<SW_AUX>(ws, (unsigned)WS_WO + (unsigned)gwo * 1024 + l * 16, o);
        }
        if (w == 2 && l < 16) {
            const u32x4 poison = {0xFFFFFFFFu, 0xFFFFFFFFu, 0xFFFFFFFFu, 0xFFFFFFFFu};
            st_ws<SW_AUX>(ws, (unsigned)WS_KVZ + (unsigned)b * 256 + l * 16, poison);
        }
        const unsigned sp = ((l31 >> 2) & 1) ? 0x80008000u : 0u;
#pragma unroll
        for (int hh = 0; hh < 2; ++hh)
#pragma unroll
            for (int j = 0; j < 4; ++j) wf[hh][j] = ((unsigned)wpv[hh][2 * j] | ((unsigned)wpv[hh][2 * j + 1] << 16)) ^ sp;
        SB();
    }
    const unsigned swz = (unsigned)((l31 >> 1) & 7);
    const unsigned rlane = (unsigned)l31 * 128 + ((swz ^ (unsigned)h) << 4);
    const unsigned tl = (unsigned)(8 * (g4 >> 1) + q4) * 128 + (unsigned)(2 * (g4 & 1) + (p4 >> 1)) * 16 + 8 * (p4 & 1);
    const unsigned tlane0 = tl + ((unsigned)(q4 >> 1) << 6);
    const unsigned tlane1 = tl + ((unsigned)(1 ^ (q4 >> 1)) << 6);

    f32x16 aq = {}, ak = {}, av = {}, c00 = {}, c01 = {}, c10 = {}, c11 = {};
    const int wr = w & 1, wc = w >> 1;
    unsigned rA = (unsigned)sbk * 4096 + rlane, tA = 8192 + (hp ? tlane1 : tlane0);
    unsigned rB = (unsigned)wr * 8192 + rlane;
    unsigned tB0 = 16384 + (unsigned)wc * 8192 + tlane0, tB1 = 16384 + (unsigned)wc * 8192 + tlane1;
    unsigned rX0, rX1, rX2, rX3, tX0, tX1;
#define SLOT(st) (((st) % NS) * 32768)
#define SETB(st) do { \
        asm volatile("" : "+v"(rA), "+v"(tA), "+v"(rB), "+v"(tB0), "+v"(tB1)); \
        if (st_isA(st)) { rX0 = rA + SLOT(st); tX0 = tA + SLOT(st); tX1 = tX0; } \
        else            { rX0 = rB + SLOT(st); tX0 = tB0 + SLOT(st); tX1 = tB1 + SLOT(st); } \
        rX1 = rX0 ^ 32; rX2 = rX0 ^ 64; rX3 = rX0 ^ 96; \
        asm volatile("" : "+v"(rX0), "+v"(rX1), "+v"(rX2), "+v"(rX3), "+v"(tX0), "+v"(tX1)); SB(); } while (0)
    u32x4 F00, F01, F02, F03, F10, F11, F12, F13, F20, F21, F22, F23, F30, F31, F32, F33;
#define RD01(st, k) do { \
        if (st_isA(st)) { F##k##0 = lds_r128(rX##k); F##k##1 = lds_trfrag(tX0 + k * 2048); } \
        else            { F##k##0 = lds_r128(rX##k); F##k##2 = lds_trfrag(tX0 + k * 2048); } } while (0)
#define RD23(st, k) do { \
        if (st_isA(st)) { F##k##2 = lds_trfrag(tX0 + k * 2048 + 8192); F##k##3 = lds_trfrag(tX0 + k * 2048 + 16384); } \
        else            { F##k##1 = lds_r128(rX##k + 4096); F##k##3 = lds_trfrag(tX1 + k * 2048); } } while (0)
#define STEP(st, k, nst, nk) do { \
        if (st_isA(st)) { \
            aq = mfma32(F##k##1, F##k##0, aq); SB(); \
            RD01(nst, nk); SB(); \
            ak = mfma32(F##k##2, F##k##0, ak); SB(); \
            RD23(nst, nk); SB(); \
            av = mfma32(F##k##0, F##k##3, av); SB(); \
        } else { \
            c00 = mfma32(F##k##2, F##k##0, c00); SB(); \
            RD01(nst, nk); SB(); \
            c01 = mfma32(F##k##2, F##k##1, c01); SB(); \
            RD23(nst, nk); SB(); \
            c10 = mfma32(F##k##3, F##k##0, c10); SB(); \
            c11 = mfma32(F##k##3, F##k##1, c11); SB(); \
        } } while (0)
#define STAGE(i) do { STEP(i, 0, i, 2); STEP(i, 1, i, 3); SETB((i) + 1); BARRIER(); SB(); STEP(i, 2, (i) + 1, 0); STEP(i, 3, (i) + 1, 1); } while (0)
#define STAGE3(i) do { STAGE(i); STAGE((i) + 1); STAGE((i) + 2); } while (0)
    BARRIER();
    SETB(0);
    RD01(0, 0); RD23(0, 0); RD01(0, 1); RD23(0, 1);
    SB();
    STAGE3(0); STAGE3(3); STAGE3(6); STAGE3(9); STAGE3(12); STAGE3(15); STAGE3(18);
    STAGE(21);
    {
        const unsigned scr = SCR + (unsigned)w * 8192 + l * 16;
        *(LDS_AS u32x4*)(uintptr_t)(scr) = pack8(ak, 0);
        *(LDS_AS u32x4*)(uintptr_t)(scr + 1024) = pack8(ak, 8);
        *(LDS_AS u32x4*)(uintptr_t)(scr + 2048) = pack8(av, 0);
        *(LDS_AS u32x4*)(uintptr_t)(scr + 3072) = pack8(av, 8);
        *(LDS_AS u32x4*)(uintptr_t)(scr + 4096) = wf[0];
        *(LDS_AS u32x4*)(uintptr_t)(scr + 5120) = wf[1];
        asm volatile("s_waitcnt lgkmcnt(0)\n\ts_barrier" ::: "memory");
        SB();
    }
#define STAGE_NB(i) do { STEP(i, 0, i, 2); STEP(i, 1, i, 3); SETB((i) + 1); SB(); STEP(i, 2, (i) + 1, 0); STEP(i, 3, (i) + 1, 1); } while (0)
    STAGE_NB(22); STAGE_NB(23);
    {
        const auto rs = __builtin_amdgcn_make_buffer_rsrc((void*)(ws + WS_PP), 0, 8 << 20, 0x00020000);
        const unsigned o = (unsigned)((kh * 128 + 4 * tm + 2 * wr) * 32 + 8 * tn + 4 * wc) * 1024 + l * 16;
        __builtin_amdgcn_raw_buffer_store_b128(pack8(c00, 0), rs, o, 0, ST_AUX);
        __builtin_amdgcn_raw_buffer_store_b128(pack8(c00, 8), rs, o + 1024, 0, ST_AUX);
        __builtin_amdgcn_raw_buffer_store_b128(pack8(c10, 0), rs, o + 2048, 0, ST_AUX);
        __builtin_amdgcn_raw_buffer_store_b128(pack8(c10, 8), rs, o + 3072, 0, ST_AUX);
        __builtin_amdgcn_raw_buffer_store_b128(pack8(c01, 0), rs, o + 32768, 0, ST_AUX);
        __builtin_amdgcn_raw_buffer_store_b128(pack8(c01, 8), rs, o + 32768 + 1024, 0, ST_AUX);
        __builtin_amdgcn_raw_buffer_store_b128(pack8(c11, 0), rs, o + 32768 + 2048, 0, ST_AUX);
        __builtin_amdgcn_raw_buffer_store_b128(pack8(c11, 8), rs, o + 32768 + 3072, 0, ST_AUX);
    }
    {
        const int sb = 2 * rb + sbk;
#pragma unroll
        for (int hh = 0; hh < 2; ++hh) {
            const int head = 4 * hq + 2 * hp + hh;
            const f32x16 zero = {};
            u32x4 o0, o1;
            f32x16 pq = mfma32(wf[hh], pack8(aq, 8 * hh), zero);
            softmax16(pq, o0, o1);
            const auto rs = __builtin_amdgcn_make_buffer_rsrc((void*)(ws + WS_PHIQ), 0, 4 << 20, 0x00020000);
            const unsigned o = (unsigned)((sb * 32 + head) * 2) * 1024 + l * 16;
            __builtin_amdgcn_raw_buffer_store_b128(o0, rs, o, 0, ST_AUX);
            __builtin_amdgcn_raw_buffer_store_b128(o1, rs, o + 1024, 0, ST_AUX);
        }
    }
}

constexpr int K2_LDS = NS * STAGE_B;

#define OUT_AUX 16
__device__ __forceinline__ void st_out(unsigned short* p, u32x4 v) {
    if (OUT_AUX == 2) __builtin_nontemporal_store(v, (u32x4*)p);
    else if (OUT_AUX == 16) asm volatile("global_store_dwordx4 %0, %1, off sc1\n\ts_nop 1" :: "v"(p), "v"(v) : "memory");
    else *(u32x4*)p = v;
}
__device__ __forceinline__ void store_tiles64(const f32x16& c0, const f32x16& c1, unsigned patch, unsigned short* gp  , int l) {
    const int h = l >> 5, l31 = l & 31;
    const unsigned wa = patch + l31 * 144 + h * 8;
#pragma unroll
    for (int q = 0; q < 4; ++q) {
        u32x2 a = {pk2(c0[4 * q], c0[4 * q + 1]), pk2(c0[4 * q + 2], c0[4 * q + 3])};
        u32x2 b = {pk2(c1[4 * q], c1[4 * q + 1]), pk2(c1[4 * q + 2], c1[4 * q + 3])};
        *(LDS_AS u32x2*)(uintptr_t)(wa + q * 16) = a;
        *(LDS_AS u32x2*)(uintptr_t)(wa + 64 + q * 16) = b;
    }
    asm volatile("s_waitcnt lgkmcnt(0)" ::: "memory");
#pragma unroll
    for (int p = 0; p < 4; ++p) {
        const int row = 8 * p + (l >> 3), ch = l & 7;
        const u32x4 v = *(const LDS_AS u32x4*)(uintptr_t)(patch + row * 144 + ch * 16);
        st_out(gp + (size_t)row * 1024 + ch * 8, v);
    }
}

__global__ __launch_bounds__(512, 2) void k2_kernel(char* __restrict__ ws, unsigned short* __restrict__ out)
{
    extern __shared__ __attribute__((aligned(16))) char shm[];
    (void)shm;
    const int tid = threadIdx.x, l = tid & 63;
    const int w = __builtin_amdgcn_readfirstlane(tid >> 6);
    const int b = blockIdx.x, xcd = b & 7, idx = b >> 3;
    const int role = idx & 1, j16 = idx >> 1;
    const int tm = 4 * xcd + (j16 >> 2), tn = j16 & 3;
    const int h = l >> 5, l31 = l & 31;
    if (role == 0) {
        if (w >= 4) {
            const int tbl = w & 1, kp = (w >> 1) & 1;
            const char* s0 = ws + WS_PP + ((size_t)((2 * tm + tbl) * 32 + 2 * kp)) * 1024 + l * 16;
            u32x4 g[4][4];
            auto load = [&](int st) {
                u32x4* d = g[st & 3];
                const size_t o = (size_t)(st >> 3) * ((size_t)64 * 32768) + (size_t)(st & 7) * 4096;
#pragma unroll
                for (int k = 0; k < 2; ++k) { d[2 * k] = *(const u32x4*)(s0 + o + k * 1024); d[2 * k + 1] = *(const u32x4*)(s0 + ((size_t)4 << 20) + o + k * 1024); }
                SB();
            };
            auto put = [&](int st) {
                const u32x4* d = g[st & 3];
                const unsigned buf = (unsigned)(st % NS) * STAGE_B + (unsigned)tbl * 4096 + (unsigned)kp * 2048 + l * 16;
#pragma unroll
                for (int k = 0; k < 2; ++k) {
                    u32x4 r;
#pragma unroll
                    for (int j = 0; j < 4; ++j) r[j] = pk2(bflo(d[2 * k][j]) + bflo(d[2 * k + 1][j]), bfhi(d[2 * k][j]) + bfhi(d[2 * k + 1][j]));
                    *(LDS_AS u32x4*)(uintptr_t)(buf + k * 1024) = r;
                }
                SB();
            };
            load(0); load(1); load(2); load(3);
            put(0);
            WAIT_LGKM0_BARRIER();
#pragma unroll
            for (int i = 0; i < 16; ++i) {
                if (i >= 1 && i + 3 < 16) load(i + 3);
                if (i + 1 < 16) put(i + 1);
                WAIT_LGKM0_BARRIER();
            }
            return;
        }
        const char* wdb = ws + WS_WD + ((size_t)((4 * tn + w) * 32)) * 1024;
        const unsigned wdv = (unsigned)l * 16;
        auto issue = [&](int st) {
            const unsigned buf = (unsigned)(st % NS) * STAGE_B + 8192 + (unsigned)w * 4096;
            const char* src = wdb + (size_t)(st >> 3) * ((size_t)16 * 32768) + (size_t)(st & 7) * 4096;
#pragma unroll
            for (int k = 0; k < 4; ++k) glds16_asm(src + k * 1024, wdv, buf + k * 1024);
            SB();
        };
        issue(0); issue(1); issue(2); issue(3);
        const int tb = w & 1, eh = w >> 1;
        f32x16 c0 = {}, c1 = {};
        unsigned yP = (unsigned)tb * 4096 + l * 16, yW = 8192 + (unsigned)eh * 8192 + l * 16;
        unsigned yp, yw;
        u32x4 P0, W00, W01, P1, W10, W11, P2, W20, W21, P3, W30, W31;
#define Y_SETB(st) do { asm volatile("" : "+v"(yP), "+v"(yW)); yp = yP + ((st) % NS) * 32768; yw = yW + ((st) % NS) * 32768; asm volatile("" : "+v"(yp), "+v"(yw)); SB(); } while (0)
#define Y_STEP(k, nk) do { \
            c0 = mfma32(W##k##0, P##k, c0); SB(); \
            P##nk = lds_r128(yp + nk * 1024); W##nk##0 = lds_r128(yw + nk * 1024); W##nk##1 = lds_r128(yw + 4096 + nk * 1024); SB(); \
            c1 = mfma32(W##k##1, P##k, c1); SB(); } while (0)
        WAIT_VM(12);
        BARRIER();
        Y_SETB(0);
        P0 = lds_r128(yp); W00 = lds_r128(yw); W01 = lds_r128(yw + 4096);
        P1 = lds_r128(yp + 1024); W10 = lds_r128(yw + 1024); W11 = lds_r128(yw + 4096 + 1024);
        SB();
#pragma unroll
        for (int i = 0; i < 16; ++i) {
            Y_STEP(0, 2); Y_STEP(1, 3);
            Y_SETB(i + 1);
            if (i <= 12) { WAIT_VM(8); } else if (i == 13) { WAIT_VM(4); } else { WAIT_VM(0); }
            BARRIER(); SB();
            if (i + 4 < 16) issue(i + 4);
            Y_STEP(2, 0); Y_STEP(3, 1);
        }
        store_tiles64(c0, c1, (13 % NS) * STAGE_B + (unsigned)w * 4608, out + (size_t)(64 * tm + 32 * tb) * 1024 + 512 + 128 * tn + 64 * eh, l);
        return;
    }
    {
        if (w < 2) {
            const int sl = (xcd * 16 + j16) * 2 + w;
            const int head = sl >> 3, q = (sl >> 1) & 3, hq = sl & 1;
            const char* src = ws + WS_KVP + ((size_t)(head * 4 + q)) * 1024 + (32 * hq + (l <= 16 ? l : 16)) * 16;
            f32x4 v[32];
#pragma unroll
            for (int r = 0; r < 32; ++r) v[r] = *(const f32x4*)(src + (size_t)r * (32 * 4096));
            f32x4 s0 = {0.f, 0.f, 0.f, 0.f}, s1 = {0.f, 0.f, 0.f, 0.f};
#pragma unroll
            for (int r = 0; r < 16; ++r) { s0 += v[2 * r]; s1 += v[2 * r + 1]; }
            s0 += s1;
            const unsigned long long pv = (unsigned long long)pk2(s0[0], s0[1]) | ((unsigned long long)pk2(s0[2], s0[3]) << 32);
            if (l < 32) {
                unsigned long long* dst = (unsigned long long*)(ws + WS_KVZ + (size_t)(head * 2 + (q & 1)) * 1024 + (l + 32 * (q >> 1)) * 16 + 8 * hq);
                __hip_atomic_store(dst, pv, __ATOMIC_RELAXED, __HIP_MEMORY_SCOPE_AGENT);
            }
            SB();
        }
        BARRIER();
        u32x4 fph[4][4], fwo[16], fkv[4][2];
        {
            const char* PH = ws + WS_PHIQ + ((size_t)((2 * tm) * 32 + 4 * w) * 2) * 1024 + l * 16;
#pragma unroll
            for (int k = 0; k < 4; ++k) {
                fph[k][0] = *(const u32x4*)(PH + k * 2048); fph[k][1] = *(const u32x4*)(PH + k * 2048 + 1024);
                fph[k][2] = *(const u32x4*)(PH + 65536 + k * 2048); fph[k][3] = *(const u32x4*)(PH + 65536 + k * 2048 + 1024);
            }
            SB();
        }
        {
            const auto rs = __builtin_amdgcn_make_buffer_rsrc((void*)(ws + WS_KVZ), 0, 65536, 0x00020000);
            const unsigned kvo = (unsigned)(4 * w) * 2048 + l * 16;
            asm volatile("s_waitcnt vmcnt(0)" ::: "memory");
            for (int spin = 0; spin < (1 << 16); ++spin) {
                bool ok = true;
#pragma unroll
                for (int k = 0; k < 4; ++k) {
                    fkv[k][0] = __builtin_amdgcn_raw_buffer_load_b128(rs, kvo + k * 2048, 0, 16);
                    fkv[k][1] = __builtin_amdgcn_raw_buffer_load_b128(rs, kvo + k * 2048 + 1024, 0, 16);
                }
#pragma unroll
                for (int k = 0; k < 4; ++k)
#pragma unroll
                    for (int i = 0; i < 4; ++i) ok = ok && fkv[k][0][i] != 0xFFFFFFFFu && fkv[k][1][i] != 0xFFFFFFFFu;
                if (__all(ok)) break;
                __builtin_amdgcn_s_sleep(8);
            }
        }
        {
            const char* WO = ws + WS_WO + ((size_t)((4 * tn + (w & 3)) * 32 + 16 * (w >> 2))) * 1024 + l * 16;
#pragma unroll
            for (int k = 0; k < 16; ++k) fwo[k] = *(const u32x4*)(WO + k * 1024);
            SB();
        }
#pragma unroll
        for (int k = 0; k < 4; ++k) {
#pragma unroll
            for (int sbi = 0; sbi < 2; ++sbi) {
                f32x16 nd = {};
                nd = mfma32(fkv[k][0], fph[k][2 * sbi], nd);
                nd = mfma32(fkv[k][1], fph[k][2 * sbi + 1], nd);
                float den = bflo(pk2(nd[8], nd[8]) & 0xffffu);
                den = bflo(pk2(den + 1e-6f, 0.f) & 0xffffu);
                const float inv = __builtin_amdgcn_rcpf(den);
                u32x4 of;
#pragma unroll
                for (int jj = 0; jj < 4; ++jj) {
                    const unsigned u = pk2(nd[2 * jj], nd[2 * jj + 1]);
                    of[jj] = pk2(bflo(u) * inv, bfhi(u) * inv);
                }
                *(LDS_AS u32x4*)(uintptr_t)((unsigned)(sbi * 32 + 4 * w + k) * 1024 + l * 16) = of;
            }
        }
        WAIT_LGKM0_BARRIER();
        f32x16 c0 = {}, c1 = {};
        {
            const unsigned ob = (unsigned)(16 * (w >> 2)) * 1024 + l * 16;
#pragma unroll
            for (int k = 0; k < 16; ++k) {
                const u32x4 o0 = lds_r128(ob + k * 1024), o1 = lds_r128(ob + 32768 + k * 1024);
                c0 = mfma32(fwo[k], o0, c0);
                c1 = mfma32(fwo[k], o1, c1);
            }
        }
        {
            const unsigned xb = 65536 + (unsigned)(w >> 2) * (64 * 528) + (unsigned)l31 * 528 + (unsigned)(w & 3) * 128 + h * 16;
#pragma unroll
            for (int q = 0; q < 4; ++q) {
                *(LDS_AS f32x4*)(uintptr_t)(xb + q * 32) = (f32x4){c0[4 * q], c0[4 * q + 1], c0[4 * q + 2], c0[4 * q + 3]};
                *(LDS_AS f32x4*)(uintptr_t)(xb + 32 * 528 + q * 32) = (f32x4){c1[4 * q], c1[4 * q + 1], c1[4 * q + 2], c1[4 * q + 3]};
            }
            WAIT_LGKM0_BARRIER();
#pragma unroll
            for (int p = 0; p < 2; ++p) {
                const int row = 8 * w + 4 * p + (l >> 4), ch = l & 15;
                const unsigned ra = 65536 + (unsigned)row * 528 + ch * 32;
                f32x4 a0 = *(const LDS_AS f32x4*)(uintptr_t)(ra), a1 = *(const LDS_AS f32x4*)(uintptr_t)(ra + 16);
                const f32x4 b0 = *(const LDS_AS f32x4*)(uintptr_t)(ra + 64 * 528), b1 = *(const LDS_AS f32x4*)(uintptr_t)(ra + 64 * 528 + 16);
                a0 += b0; a1 += b1;
                const u32x4 o = {pk2(a0[0], a0[1]), pk2(a0[2], a0[3]), pk2(a1[0], a1[1]), pk2(a1[2], a1[3])};
                st_out(out + (size_t)(64 * tm + row) * 1024 + 128 * tn + ch * 8, o);
            }
        }
    }
}

extern "C" void kernel_launch(void* const* d_in, const int* in_sizes, int n_in,
                              void* d_out, int out_size, void* d_ws, size_t ws_size,
                              hipStream_t stream) {
    (void)in_sizes; (void)n_in; (void)out_size; (void)ws_size;
    (void)hipFuncSetAttribute((const void*)k1_kernel, hipFuncAttributeMaxDynamicSharedMemorySize, K1_LDS);
    (void)hipFuncSetAttribute((const void*)k2_kernel, hipFuncAttributeMaxDynamicSharedMemorySize, K2_LDS);
    k1_kernel<<<256, 512, K1_LDS, stream>>>(
        (const char*)d_in[0], (const char*)d_in[1], (const unsigned short*)d_in[2], (const unsigned short*)d_in[3],
        (const char*)d_in[4], (const char*)d_in[5], (const char*)d_in[6], (const char*)d_in[7], (const char*)d_in[8],
        (char*)d_ws);
    k2_kernel<<<256, 512, K2_LDS, stream>>>((char*)d_ws, (unsigned short*)d_out);
}
```
